```python
import torch

def _softmax(x, axis=-1):
    return torch.softmax(x, dim=axis)
def _gelu(x):
    return torch.nn.functional.gelu(x, approximate="tanh")
def _silu(x):
    return torch.nn.functional.silu(x)
def _sigmoid(x):
    return torch.sigmoid(x)
def _layernorm(x, axis=-1, eps=1e-5):
    m = x.mean(dim=axis, keepdim=True)
    v = x.var(dim=axis, keepdim=True, unbiased=False)
    return (x - m) / torch.sqrt(v + eps)
def _scatter_add(tgt, idx, src, axis=0):
    return tgt.clone().index_add_(axis, idx, src)
def _segment_sum(src, seg, n):
    out = torch.zeros((n,) + src.shape[1:], dtype=src.dtype)
    return out.index_add_(0, seg, src)
def _pad(x, pw):
    flat = tuple(v for pair in reversed(pw) for v in pair)
    return torch.nn.functional.pad(x, flat)

kernel_name = 'gated_mix__spectral_fnet__dense_activation_relu__prompt'

def setup_inputs():

    _w = (lambda a: (lambda t: t.to(torch.bfloat16) if t.is_floating_point() else t)(torch.as_tensor(a)))
    def _A_setup():
        B, S, D = 1, 2048, 576
        ns = torch.arange(S)[:, None] * torch.arange(S)[None, :]
        nd = torch.arange(D)[:, None] * torch.arange(D)[None, :]
        Ws_re = torch.cos(-2.0 * torch.pi * ns / S).to(float)
        Ws_im = torch.sin(-2.0 * torch.pi * ns / S).to(float)
        Wd_re = torch.cos(-2.0 * torch.pi * nd / D).to(float)
        Wd_im = torch.sin(-2.0 * torch.pi * nd / D).to(float)
        return {
            "x": _w((lambda *s: torch.randn(*s, dtype=torch.bfloat16) * 0.05)(B, S, D)),
            "dft_seq_re": _w(Ws_re),
            "dft_seq_im": _w(Ws_im),
            "dft_hid_re": _w(Wd_re),
            "dft_hid_im": _w(Wd_im),
        }
    _A_in = _A_setup()

    def _B_setup():
        B, S, D, Dff = 1, 2048, 576, 4608
        return {
            "x": _w((lambda *s: torch.randn(*s, dtype=torch.bfloat16) * 0.05)(B, S, D)),
            "w1": _w((lambda *s: torch.randn(*s, dtype=torch.bfloat16) * 0.4)(D, Dff)),
            "w2": _w((lambda *s: torch.randn(*s, dtype=torch.bfloat16) * 0.4)(Dff, D)),
        }
    _B_in = _B_setup()

    return {
        "A_x": _A_in["x"],
        "A_dft_seq_re": _A_in["dft_seq_re"],
        "A_dft_seq_im": _A_in["dft_seq_im"],
        "A_dft_hid_re": _A_in["dft_hid_re"],
        "A_dft_hid_im": _A_in["dft_hid_im"],
        "B_x": _B_in["x"],
        "B_w1": _B_in["w1"],
        "B_w2": _B_in["w2"],
        "gate_w": _w((lambda *s: torch.randn(*s, dtype=torch.bfloat16) * 0.05)(576) * 16.0),
    }

def reference(A_x, A_dft_seq_re, A_dft_seq_im, A_dft_hid_re, A_dft_hid_im, B_x, B_w1, B_w2, gate_w):
    def _A_ref(x, dft_seq_re, dft_seq_im, dft_hid_re, dft_hid_im):
        a = torch.einsum("bsd,ed->bse", x, dft_hid_re)
        b = torch.einsum("bsd,ed->bse", x, dft_hid_im)
        y = torch.einsum("ts,bse->bte", dft_seq_re, a) - torch.einsum("ts,bse->bte", dft_seq_im, b)
        return y

    def _B_ref(x, w1, w2):
        h = torch.einsum("bsd,df->bsf", x, w1)
        zero = torch.zeros_like(h)
        h = torch.maximum(h, zero)
        return torch.einsum("bsf,fd->bsd", h, w2)

    x = A_x
    g = _sigmoid((x * gate_w).sum(-1, keepdims=True))
    a = _A_ref(x, A_dft_seq_re, A_dft_seq_im, A_dft_hid_re, A_dft_hid_im)
    b = _B_ref(x, B_w1, B_w2)
    return g * a + (1 - g) * b
```

```cpp
#include <hip/hip_runtime.h>
#include <hip/hip_bf16.h>
#include <stdint.h>

typedef __attribute__((ext_vector_type(8))) short bf16x8;
typedef __attribute__((ext_vector_type(4))) short s16x4;
typedef __attribute__((ext_vector_type(4))) float f32x4;
typedef __attribute__((ext_vector_type(2))) float f32x2;
typedef __attribute__((ext_vector_type(4))) unsigned u32x4;
typedef __attribute__((ext_vector_type(2))) unsigned u32x2;
typedef __attribute__((ext_vector_type(2))) __bf16 bf16x2_t;
#define AS3 __attribute__((address_space(3)))
#define AS1 __attribute__((address_space(1)))
typedef __amdgpu_buffer_rsrc_t rsrc_t;

namespace {
constexpr int S = 2048, D = 576, F = 4608;
constexpr int WS_EPOCH = 0;
constexpr int WS_FA = 4096;
constexpr int WS_FH = 8192;
constexpr int WS_FX = 12288;
constexpr int WS_G = 32768;
constexpr int WS_PROF = 64 << 20;
constexpr int WS_AB = 1 << 20;
constexpr int WS_XP = 6 << 20;
constexpr int WS_H = 16 << 20;
constexpr int WS_END = WS_H + 128 * 144 * 1024;
constexpr int HS = 26624;
constexpr int BS = 53248;
constexpr int LDS_BYTES = 3 * BS;
constexpr int BIMG = 18432;
constexpr unsigned SPIN_MAX = 1u << 22;
constexpr int AUX_SC1 = 16;

struct Params {
  const void* x; const void* wsr; const void* wsi; const void* wdr; const void* wdi;
  const void* w1; const void* w2; const void* gw; void* out; void* ws;
};

__device__ __forceinline__ rsrc_t mk(const void* p, int bytes) {
  return __builtin_amdgcn_make_buffer_rsrc((void*)p, (short)0, bytes, 0x00020000);
}
__device__ __forceinline__ int cslot(int s) {
  int i = s & 3;
  return i == 0 ? 124928 : (i == 1 ? 0 : (i == 2 ? 53248 : 88064));
}
__device__ __forceinline__ unsigned pk(float a, float b) {
  f32x2 v = {a, b};
  return __builtin_bit_cast(unsigned, __builtin_convertvector(v, bf16x2_t));
}
__device__ __forceinline__ float blo(unsigned u) { return __uint_as_float(u << 16); }
__device__ __forceinline__ float bhi(unsigned u) { return __uint_as_float(u & 0xffff0000u); }
__device__ __forceinline__ float rbf(float a) { return blo(pk(a, 0.f)); }
__device__ __forceinline__ bf16x8 ld128(AS3 char* p) { return *(AS3 bf16x8*)p; }
__device__ __forceinline__ bf16x8 ldtr(AS3 char* p) {
  s16x4 lo = __builtin_amdgcn_ds_read_tr16_b64_v4i16((AS3 s16x4*)p);
  s16x4 hi = __builtin_amdgcn_ds_read_tr16_b64_v4i16((AS3 s16x4*)(p + 4608));
  return __builtin_shufflevector(lo, hi, 0, 1, 2, 3, 4, 5, 6, 7);
}
#define MFMA(a, b, c) __builtin_amdgcn_mfma_f32_16x16x32_bf16(a, b, c, 0, 0, 0)
#define MFMA_ACC(c, a, b) asm volatile("v_mfma_f32_16x16x32_bf16 %0, %1, %2, %0" : "+v"(c) : "v"(a), "v"(b))
#define NOP_MFMA_READ() do { SCHED(); asm volatile("s_nop 7\n\ts_nop 3"); SCHED(); } while (0)
#define NOP_VALU_WRITE() do { SCHED(); asm volatile("s_nop 3"); SCHED(); } while (0)
#define BAR() do { asm volatile("" ::: "memory"); __builtin_amdgcn_s_barrier(); asm volatile("" ::: "memory"); } while (0)
#define SCHED() __builtin_amdgcn_sched_barrier(0)
#define WAIT_LGKM0() __builtin_amdgcn_s_waitcnt(0xC07F)
template <int N> __device__ __forceinline__ void wait_vm() {
  asm volatile("s_waitcnt vmcnt(%0)" ::"n"(N) : "memory");
}
template <int AUX = 0>
__device__ __forceinline__ void dma(rsrc_t r, AS3 char* l, int voff, int soff) {
  __builtin_amdgcn_raw_ptr_buffer_load_lds(r, (AS3 void*)l, 16, voff, soff, 0, AUX);
}
__device__ __forceinline__ u32x4 swap_pair(const f32x4& a, const f32x4& b) {
  unsigned x0 = pk(a[0], a[1]), x1 = pk(a[2], a[3]), y0 = pk(b[0], b[1]), y1 = pk(b[2], b[3]);
  auto s0 = __builtin_amdgcn_permlane16_swap(x0, y0, false, false);
  auto s1 = __builtin_amdgcn_permlane16_swap(x1, y1, false, false);
  u32x4 v = {s0[0], s1[0], s0[1], s1[1]};
  return v;
}
__device__ __forceinline__ unsigned poll_issue(const AS1 unsigned* p) {
  unsigned f;
  asm volatile("global_load_dword %0, %1, off sc1" : "=v"(f) : "v"(p) : "memory");
  return f;
}
__device__ __forceinline__ void poll_check(unsigned f, const AS1 unsigned* p, unsigned tag) {
  asm volatile("" : "+v"(f));
  unsigned spins = 0;
  while (!__all(f == tag)) {
    __builtin_amdgcn_s_sleep(2);
    asm volatile("global_load_dword %0, %1, off sc1\n\ts_waitcnt vmcnt(0)" : "=&v"(f) : "v"(p) : "memory");
    if (++spins > SPIN_MAX) break;
  }
}
__device__ __forceinline__ void flag_store(AS1 unsigned* p, unsigned v) {
  __hip_atomic_store(p, v, __ATOMIC_RELAXED, __HIP_MEMORY_SCOPE_AGENT);
}

#define RSTAMP(i) do {} while (0)
#define STAMP(i) do {} while (0)

struct Ids { int wg, rho, sig, loc, rb64, pc, rb256, j, rb128, cb; };

#define LBAR() BAR()
template <int NB>
__device__ __forceinline__ void loader(const Params& P, const Ids& I, AS3 char* L, int lw, int lane, unsigned tag) {
  const int wg = I.wg, wave = lw + 4;
  (void)wg; (void)wave;
  constexpr int N1 = 2 + NB, N2 = 8 + NB, N3 = 4 + NB;
  const rsrc_t rx = mk(P.x, S * D * 2);
  const rsrc_t rwd = mk((I.pc >> 2) ? P.wdi : P.wdr, D * D * 2);
  const rsrc_t rw1 = mk(P.w1, D * F * 2);
  const rsrc_t rw2 = mk(P.w2, F * D * 2);
  const rsrc_t rs = mk((I.sig >> 1) ? P.wsi : P.wsr, S * S * 2);
  const rsrc_t rws = mk(P.ws, WS_END);
  const int sw = ((lane & 7) ^ (((lw & 1) << 2) + (lane >> 4))) << 4;
  const int vx = (lw * 8 + (lane >> 3)) * (D * 2) + sw;
  const int vs = (lw * 8 + (lane >> 3)) * (S * 2) + sw;
  const int vl = lane * 16;
  int v1[NB], v2[NB];
#pragma unroll
  for (int i = 0; i < NB; ++i) {
    int c = (lw + 4 * i) * 64 + lane, r = c / 18, col = c - r * 18;
    int k = (r & 32) + ((r >> 2) & 3) * 8 + ((r >> 4) & 1) * 4 + (r & 3);
    v1[i] = k * (F * 2) + col * 16;
    v2[i] = k * (D * 2) + col * 16;
  }
  auto issueA = [&](int st) {
    AS3 char* b = L + (st % 6) * HS + lw * 1024;
#pragma unroll
    for (int i = 0; i < 2; ++i) dma(rx, b + i * 4096, vx, (I.rb64 * 64 + i * 32) * (D * 2) + st * 128);
#pragma unroll
    for (int i = 0; i < NB; ++i) dma(rwd, b + 8192 + i * 4096, vx, ((I.pc & 3) * 144 + i * 32) * (D * 2) + st * 128);
  };
  auto issueB = [&](int st) {
    AS3 char* b = L + ((st + 2) % 3) * BS + lw * 1024;
#pragma unroll
    for (int i = 0; i < NB; ++i) dma(rw1, b + 32768 + i * 4096, v1[i], st * 64 * (F * 2) + (I.sig * 8 + I.j) * 288);
#pragma unroll
    for (int i = 0; i < 8; ++i) dma(rx, b + i * 4096, vx, (I.rb256 * 256 + i * 32) * (D * 2) + st * 128);
  };
  auto issueC = [&](int st) {
    AS3 char* b = L + cslot(st) + lw * 1024;
#pragma unroll
    for (int i = 0; i < 4; ++i) dma(rs, b + i * 4096, vs, (I.rb128 * 128 + i * 32) * (S * 2) + (I.sig & 1) * 2048 + st * 128);
#pragma unroll
    for (int i = 0; i < NB; ++i) dma<AUX_SC1>(rws, b + 16384 + i * 4096, vl, WS_AB + ((I.sig * 16 + st) * 4 + I.cb) * BIMG + (lw + 4 * i) * 1024);
  };
  auto issueD = [&](int st) {
    AS3 char* b = L + cslot(st) + lw * 1024;
    const int t = st - 16;
#pragma unroll
    for (int i = 0; i < NB; ++i) dma(rw2, b + 16384 + i * 4096, v2[i], (I.sig * 1152 + t * 64) * (D * 2) + I.cb * 288);
#pragma unroll
    for (int i = 0; i < 4; ++i)
      dma<AUX_SC1>(rws, b + i * 4096, vl, WS_H + ((I.rb128 * 8 + 2 * i + (lw >> 1)) * 144 + I.sig * 36 + 2 * t + (lw & 1)) * 1024);
  };
  AS1 unsigned* wsu = (AS1 unsigned*)P.ws;

  STAMP(0); RSTAMP(13);
#pragma unroll 1
  for (int st = 0; st < 6; ++st) issueA(st);
  asm volatile("" ::"s"(tag));
  int bi = 0; (void)bi;
  wait_vm<5 * N1>(); LBAR();
  STAMP(1);
  wait_vm<4 * N1>(); LBAR(); issueA(6);
  wait_vm<4 * N1>(); LBAR(); issueA(7);
  wait_vm<4 * N1>(); LBAR(); issueA(8);
  wait_vm<4 * N1>(); LBAR();
  wait_vm<3 * N1>(); LBAR();
  wait_vm<2 * N1>(); LBAR(); issueB(0);
  wait_vm<N1 + N2>(); LBAR();
  wait_vm<N2>(); LBAR(); issueB(1);
  STAMP(2);
  wait_vm<N2>(); LBAR(); issueB(2);
  const AS1 unsigned* pfa = wsu + WS_FA / 4 + I.cb * 64 + I.sig * 16 + (lane & 15);
  unsigned f = 0;
#pragma unroll 1
  for (int st = 3; st < 9; ++st) {
    wait_vm<N2>(); LBAR();
    if (st == 5 && lw == 0 && lane == 0) flag_store(wsu + WS_FA / 4 + (I.pc & 3) * 64 + (I.pc >> 2) * 32 + I.rb64, tag);
    if (st == 8) f = poll_issue(pfa);
    issueB(st);
  }
  wait_vm<N2>(); LBAR();
  poll_check(f, pfa, tag);
  issueC(0);
  wait_vm<N3>(); LBAR(); issueC(1);
  LBAR(); issueC(2); issueC(3);
  STAMP(3);
  wait_vm<3 * N3>(); LBAR();
  const AS1 unsigned* pfh = wsu + WS_FH / 4 + I.rb256 * 32 + I.sig * 8 + (lane & 7);
#pragma unroll 1
  for (int u = 0; u < 12; ++u) {
    wait_vm<2 * N3>(); LBAR();
    if (u == 8 && lw == 0 && lane == 0) flag_store(wsu + WS_FH / 4 + I.rb256 * 32 + I.sig * 8 + I.j, tag);
    if (u == 10) f = poll_issue(pfh);
    issueC(u + 4);
  }
  wait_vm<2 * N3>(); LBAR();
  STAMP(4);
  poll_check(f, pfh, tag);
  STAMP(5);
  issueD(16);
#pragma unroll 1
  for (int u = 13; u < 30; ++u) {
    wait_vm<2 * N3>(); LBAR();
    issueD(u + 4);
  }
  wait_vm<2 * N3>(); LBAR();
  wait_vm<N3>(); LBAR();
  wait_vm<0>(); LBAR();
  LBAR();
  STAMP(6);
  if (I.wg == 0 && lw == 1) {
    const AS1 unsigned* pa = wsu + WS_FA / 4 + lane;
#pragma unroll 1
    for (int i = 0; i < 4; ++i) poll_check(0u - (tag == 0u), pa + i * 64, tag);
    if (lane == 0) flag_store(wsu + WS_EPOCH / 4, tag);
  }
  BAR();
  BAR();
  if (lw == 0) {
    const int grp = I.loc * 8 + I.rho * 4;
    if (lane < 4 && lane != I.sig) flag_store(wsu + WS_FX / 4 + (grp + lane) * 4 + I.sig, tag);
    const int src = (lane & 3) == I.sig ? ((I.sig + 1) & 3) : (lane & 3);
    poll_check(0u - (tag == 0u), wsu + WS_FX / 4 + I.wg * 4 + src, tag);
  }
  STAMP(7);
}

__device__ __forceinline__ void unit(f32x4 (&acc)[4][9], const bf16x8 (&Ac)[4], bf16x8 (&An)[4], bf16x8 (&B)[7],
                                     const bf16x8 (&Xc)[2], bf16x8 (&Xn)[2], AS3 char* a, AS3 char* b) {
  SCHED();
#pragma unroll
  for (int n = 0; n < 7; ++n) {
    MFMA_ACC(acc[0][n], B[n], Ac[0]);
    SCHED();
    if (n < 4) An[n] = ld128(a + n * 2048);
    SCHED();
    MFMA_ACC(acc[1][n], B[n], Ac[1]);
    SCHED();
    if (n < 2) Xn[n] = ldtr(b + (7 + n) * 32);
    SCHED();
    MFMA_ACC(acc[2][n], B[n], Ac[2]);
    MFMA_ACC(acc[3][n], B[n], Ac[3]);
    SCHED();
    B[n] = ldtr(b + n * 32);
    SCHED();
  }
#pragma unroll
  for (int n = 0; n < 2; ++n)
#pragma unroll
    for (int m = 0; m < 4; ++m) MFMA_ACC(acc[m][7 + n], Xc[n], Ac[m]);
  SCHED();
  WAIT_LGKM0();
}
__device__ __forceinline__ void first_read(bf16x8 (&A)[4], bf16x8 (&B)[7], bf16x8 (&X)[2], AS3 char* a, AS3 char* b) {
#pragma unroll
  for (int m = 0; m < 4; ++m) A[m] = ld128(a + m * 2048);
#pragma unroll
  for (int n = 0; n < 7; ++n) B[n] = ldtr(b + n * 32);
#pragma unroll
  for (int n = 0; n < 2; ++n) X[n] = ldtr(b + (7 + n) * 32);
  WAIT_LGKM0();
}
__device__ __forceinline__ void load_g(float (&g)[4], const Params& P, int row0, int r, unsigned tag) {
  const AS1 unsigned long long* gp = (const AS1 unsigned long long*)((AS1 char*)P.ws + WS_G) + row0 + r;
  for (unsigned spins = 0;; ++spins) {
    bool ok = true;
#pragma unroll
    for (int m = 0; m < 4; ++m) {
      unsigned long long v = __hip_atomic_load(gp + m * 16, __ATOMIC_RELAXED, __HIP_MEMORY_SCOPE_AGENT);
      g[m] = __uint_as_float((unsigned)v);
      ok &= (unsigned)(v >> 32) == tag;
    }
    if (__all(ok) || spins > SPIN_MAX) break;
    __builtin_amdgcn_s_sleep(2);
  }
}
template <int ODD>
__device__ __forceinline__ void store_h(const f32x4 (&acc)[4][9], const float (&om)[4], rsrc_t rws, int sbase, int r, int q) {
  const int vp = ((((q & 1) << 1) | (q >> 1)) * 16 + r) * 16 + sbase + ODD * 1024;
  const int vh = ((ODD * 2 + (q >> 1)) * 16 + r) * 16 + (q & 1) * 8 + sbase + (ODD ? 0 : 4096);
#pragma unroll
  for (int m = 0; m < 4; ++m) {
    f32x4 t[9];
#pragma unroll
    for (int n = 0; n < 9; ++n)
#pragma unroll
      for (int e = 0; e < 4; ++e) t[n][e] = fmaxf(acc[m][n][e], 0.f) * om[m];
#pragma unroll
    for (int p = 0; p < 4; ++p)
      __builtin_amdgcn_raw_buffer_store_b128(swap_pair(t[2 * p + ODD], t[2 * p + 1 + ODD]), rws,
                                             vp + m * 147456 + p * 1024, 0, AUX_SC1);
    const f32x4 s = t[ODD ? 0 : 8];
    u32x2 v = {pk(s[0], s[1]), pk(s[2], s[3])};
    __builtin_amdgcn_raw_buffer_store_b64(v, rws, vh + m * 147456, 0, AUX_SC1);
  }
}

__device__ __forceinline__ void consumer(const Params& P, const Ids& I, AS3 char* L, int cw, int lane, unsigned tag) {
  const int wg = I.wg, wave = cw;
  (void)wg; (void)wave;
  const int r = lane & 15, q = lane >> 4;
  const rsrc_t rws = mk(P.ws, WS_END);
  STAMP(0);
  {
    const int row = I.rb64 * 64 + I.pc * 8 + cw * 2 + (lane >> 5), l32 = lane & 31;
    const u32x4* xr = (const u32x4*)((const char*)P.x + row * (D * 2));
    const u32x4* gw = (const u32x4*)P.gw;
    float s = 0.f;
#pragma unroll
    for (int i = 0; i < 3; ++i) {
      const int c = l32 + 32 * i;
      const bool on = c < 72;
      const u32x4 a = xr[on ? c : 0], w = gw[on ? c : 0];
      float t = 0.f;
#pragma unroll
      for (int e = 0; e < 4; ++e) t += rbf(blo(a[e]) * blo(w[e])) + rbf(bhi(a[e]) * bhi(w[e]));
      s += on ? t : 0.f;
    }
#pragma unroll
    for (int o = 16; o > 0; o >>= 1) s += __shfl_xor(s, o);
    const float g = rbf(1.f / (1.f + __expf(-rbf(s))));
    if (l32 == 0)
      __hip_atomic_store((AS1 unsigned long long*)((AS1 char*)P.ws + WS_G) + row,
                         ((unsigned long long)tag << 32) | __float_as_uint(g), __ATOMIC_RELAXED, __HIP_MEMORY_SCOPE_AGENT);
  }
  const int aL0 = r * 128 + ((q ^ ((r >> 1) & 7)) << 4), aL1 = aL0 ^ 64;
  const int bL = (4 * q + ((lane >> 2) & 3)) * 288 + (lane & 3) * 8;
  f32x4 acc[4][9];
  {
#pragma unroll
    for (int n = 0; n < 9; ++n) acc[0][n] = f32x4{0.f, 0.f, 0.f, 0.f};
    int hs = 0;
#pragma unroll 1
    for (int st = 0; st < 9; ++st) {
      BAR();
      if (st == 0) STAMP(1);
      AS3 char* b0 = L + hs + aL0;
      AS3 char* b1 = L + hs + aL1;
      bf16x8 xa = ld128(b0 + cw * 2048), xb = ld128(b1 + cw * 2048), w0[9], w1[9];
#pragma unroll
      for (int n = 0; n < 9; ++n) w0[n] = ld128(b0 + 8192 + n * 2048);
#pragma unroll
      for (int n = 0; n < 9; ++n) w1[n] = ld128(b1 + 8192 + n * 2048);
#pragma unroll
      for (int n = 0; n < 9; ++n) acc[0][n] = MFMA(w0[n], xa, acc[0][n]);
#pragma unroll
      for (int n = 0; n < 9; ++n) acc[0][n] = MFMA(w1[n], xb, acc[0][n]);
      WAIT_LGKM0();
      hs = (st == 5) ? 0 : hs + HS;
    }
    STAMP(2);
    const float sg = (I.pc >> 2) ? -1.f : 1.f;
    const int sl = cw * 16 + r, k = sl & 31;
    const int rho = (sl & 32) + ((k >> 2) & 1) * 16 + (k >> 3) * 4 + (k & 3);
    const int sb = WS_AB + (((I.pc >> 2) * 32 + I.rb64) * 4 + (I.pc & 3)) * BIMG;
    const int vp = sb + rho * 288 + (((q & 1) << 1) | (q >> 1)) * 16;
#pragma unroll
    for (int p = 0; p < 4; ++p)
      __builtin_amdgcn_raw_buffer_store_b128(swap_pair(acc[0][2 * p] * sg, acc[0][2 * p + 1] * sg), rws, vp + p * 64, 0, AUX_SC1);
    const f32x4 s = acc[0][8] * sg;
    u32x2 v = {pk(s[0], s[1]), pk(s[2], s[3])};
    __builtin_amdgcn_raw_buffer_store_b64(v, rws, sb + rho * 288 + 256 + q * 8, 0, AUX_SC1);
  }
  bf16x8 A0[4], A1[4], B[7], X0[2], X1[2];
  {
#pragma unroll
    for (int m = 0; m < 4; ++m)
#pragma unroll
      for (int n = 0; n < 9; ++n) { acc[m][n] = f32x4{0.f, 0.f, 0.f, 0.f}; asm volatile("" : "+v"(acc[m][n])); }
    const int a0 = aL0 + cw * 8192, a1 = aL1 + cw * 8192, b0 = bL + 32768;
    BAR();
    STAMP(3);
    int sl = 2 * BS;
    first_read(A0, B, X0, L + sl + a0, L + sl + b0);
    NOP_VALU_WRITE();
#pragma unroll 1
    for (int st = 0; st < 9; ++st) {
      if (st == 2) wait_vm<0>();
      unit(acc, A0, A1, B, X0, X1, L + sl + a1, L + sl + b0 + 9216);
      BAR();
      sl = (sl == 2 * BS) ? 0 : sl + BS;
      unit(acc, A1, A0, B, X1, X0, L + sl + a0, L + sl + b0);
    }
    NOP_MFMA_READ();
    STAMP(4);
    float om[4];
    load_g(om, P, I.rb256 * 256 + cw * 64, r, tag);
#pragma unroll
    for (int m = 0; m < 4; ++m) om[m] = rbf(1.f - om[m]);
    const int sb = WS_H + ((I.rb256 * 16 + cw * 4) * 144 + I.sig * 36 + ((I.j * 9) >> 1)) * 1024;
    if (I.j & 1) store_h<1>(acc, om, rws, sb, r, q);
    else store_h<0>(acc, om, rws, sb, r, q);
    STAMP(5);
  }
  const int mh = cw >> 1, kg = cw & 1;
  {
    float g[4];
    load_g(g, P, I.rb128 * 128 + mh * 64, r, tag);
#pragma unroll
    for (int m = 0; m < 4; ++m)
#pragma unroll
      for (int n = 0; n < 9; ++n) { acc[m][n] = f32x4{0.f, 0.f, 0.f, 0.f}; asm volatile("" : "+v"(acc[m][n])); }
    const int aS = (kg ? aL1 : aL0) + mh * 8192, aH = mh * 8192 + kg * 1024 + lane * 16;
    const int bb = bL + 16384 + kg * 9216;
    BAR();
    STAMP(6);
    first_read(A0, B, X0, L + cslot(0) + aS, L + cslot(0) + bb);
    NOP_VALU_WRITE();
#pragma unroll 1
    for (int u = 0; u < 16; u += 2) {
      if (u == 8) wait_vm<0>();
      BAR();
      const int s1 = cslot(u + 1), s2 = cslot(u + 2);
      unit(acc, A0, A1, B, X0, X1, L + s1 + aS, L + s1 + bb);
      BAR();
      unit(acc, A1, A0, B, X1, X0, L + s2 + (u < 14 ? aS : aH), L + s2 + bb);
    }
    NOP_MFMA_READ();
    STAMP(7);
#pragma unroll
    for (int m = 0; m < 4; ++m)
#pragma unroll
      for (int n = 0; n < 9; ++n) { acc[m][n] *= g[m]; asm volatile("" : "+v"(acc[m][n])); }
    NOP_VALU_WRITE();
#pragma unroll 1
    for (int u = 16; u < 34; u += 2) {
      BAR();
      const int s1 = cslot(u + 1), s2 = cslot(u + 2);
      unit(acc, A0, A1, B, X0, X1, L + s1 + aH, L + s1 + bb);
      BAR();
      unit(acc, A1, A0, B, X1, X0, L + s2 + aH, L + s2 + bb);
    }
    NOP_MFMA_READ();
    STAMP(8);
  }
  {
    AS3 char* wr = L + cw * BIMG + lane * 16;
    AS3 char* rd = L + (cw ^ 1) * BIMG + lane * 16;
    f32x4 keep[2][9];
#pragma unroll
    for (int m = 0; m < 2; ++m)
#pragma unroll
      for (int n = 0; n < 9; ++n) {
        *(AS3 f32x4*)(wr + (m * 9 + n) * 1024) = kg ? acc[m][n] : acc[2 + m][n];
        keep[m][n] = kg ? acc[2 + m][n] : acc[m][n];
      }
    WAIT_LGKM0();
    BAR();
#pragma unroll
    for (int m = 0; m < 2; ++m)
#pragma unroll
      for (int n = 0; n < 9; ++n) keep[m][n] += *(AS3 f32x4*)(rd + (m * 9 + n) * 1024);
    if (cw == I.sig) {
      AS3 char* kp = L + 4 * BIMG + lane * 16;
#pragma unroll
      for (int m = 0; m < 2; ++m) {
#pragma unroll
        for (int n = 0; n < 8; ++n) *(AS3 f32x4*)(kp + (m * 8 + n) * 1024) = keep[m][n];
        *(AS3 f32x4*)(kp + (16 + m) * 1024) = keep[m][8];
      }
      WAIT_LGKM0();
    } else {
      const int sb = WS_XP + ((I.loc * 8 + I.rho * 4 + cw) * 4 + I.sig) * 9216;
#pragma unroll
      for (int it = 0; it < 9; ++it) {
        const f32x4 a = it < 8 ? keep[it >> 2][(it & 3) * 2] : keep[0][8];
        const f32x4 b = it < 8 ? keep[it >> 2][(it & 3) * 2 + 1] : keep[1][8];
        u32x4 v = {pk(a[0], a[1]), pk(a[2], a[3]), pk(b[0], b[1]), pk(b[2], b[3])};
        __builtin_amdgcn_raw_buffer_store_b128(v, rws, sb + lane * 16 + it * 1024, 0, AUX_SC1);
      }
      wait_vm<0>();
    }
    STAMP(9);
    BAR();
  }
}

__device__ __forceinline__ void final_item(const Params& P, const Ids& I, AS3 char* L, int it, int lane) {
  const rsrc_t rws = mk(P.ws, WS_END);
  const rsrc_t ro = mk(P.out, S * D * 2);
  const int r = lane & 15, q = lane >> 4;
  u32x4 p[3];
#pragma unroll
  for (int i = 0; i < 3; ++i) {
    const int src = (I.sig + 1 + i) & 3;
    p[i] = __builtin_amdgcn_raw_buffer_load_b128(rws, lane * 16, WS_XP + (I.wg * 4 + src) * 9216 + it * 1024, AUX_SC1);
  }
  f32x4 a = *(AS3 f32x4*)(L + 4 * BIMG + it * 2048 + lane * 16);
  f32x4 b = *(AS3 f32x4*)(L + 4 * BIMG + it * 2048 + 1024 + lane * 16);
#pragma unroll
  for (int i = 0; i < 3; ++i) {
    a += f32x4{blo(p[i][0]), bhi(p[i][0]), blo(p[i][1]), bhi(p[i][1])};
    b += f32x4{blo(p[i][2]), bhi(p[i][2]), blo(p[i][3]), bhi(p[i][3])};
  }
  const int row0 = I.rb128 * 128 + I.sig * 32, col0 = I.cb * 144;
  if (it < 8) {
    const int vo = r * (D * 2) + (((q & 1) << 1) | (q >> 1)) * 16;
    __builtin_amdgcn_raw_buffer_store_b128(swap_pair(a, b), ro, vo + (row0 + (it >> 2) * 16) * (D * 2) + (col0 + (it & 3) * 32) * 2, 0, 0);
  } else {
    const int vo = r * (D * 2) + q * 8;
    u32x2 va = {pk(a[0], a[1]), pk(a[2], a[3])}, vb = {pk(b[0], b[1]), pk(b[2], b[3])};
    __builtin_amdgcn_raw_buffer_store_b64(va, ro, vo + row0 * (D * 2) + (col0 + 128) * 2, 0, 0);
    __builtin_amdgcn_raw_buffer_store_b64(vb, ro, vo + (row0 + 16) * (D * 2) + (col0 + 128) * 2, 0, 0);
  }
}
}

__global__ __launch_bounds__(512, 2) void gated_mix_fnet_ffn_kernel(Params P) {
  __shared__ __attribute__((aligned(1024))) char lds[LDS_BYTES];
  AS3 char* L = (AS3 char*)lds;
  const int lane = threadIdx.x & 63;
  const int wave = __builtin_amdgcn_readfirstlane(threadIdx.x >> 6);
  Ids I;
  I.wg = blockIdx.x;
  I.rho = (I.wg >> 2) & 1; I.sig = I.wg & 3; I.loc = I.wg >> 3;
  I.rb64 = 16 * I.rho + (I.loc >> 1); I.pc = 2 * I.sig + (I.loc & 1);
  I.rb256 = 4 * I.rho + (I.loc >> 3); I.j = I.loc & 7;
  I.rb128 = 2 * I.rb256 + (I.j & 1); I.cb = I.j >> 1;
  const unsigned tag = *(const __attribute__((address_space(4))) unsigned*)((uintptr_t)P.ws + WS_EPOCH) + 1u;
  if (wave < 4) consumer(P, I, L, wave, lane, tag);
  else if (wave < 6) loader<5>(P, I, L, wave - 4, lane, tag);
  else loader<4>(P, I, L, wave - 4, lane, tag);
  BAR();
  {
    int tz = threadIdx.x;
    asm volatile("" : "+v"(tz));
    const int w2 = __builtin_amdgcn_readfirstlane(tz >> 6);
#pragma unroll 1
    for (int it = w2; it < 9; it += 8) final_item(P, I, L, it, tz & 63);
  }
}

extern "C" void kernel_launch(void* const* d_in, const int* in_sizes, int n_in, void* d_out, int out_size, void* d_ws,
                              size_t ws_size, hipStream_t stream) {
  Params P;
  P.x = d_in[0]; P.wsr = d_in[1]; P.wsi = d_in[2]; P.wdr = d_in[3]; P.wdi = d_in[4];
  P.w1 = d_in[6]; P.w2 = d_in[7]; P.gw = d_in[8]; P.out = d_out; P.ws = d_ws;
  gated_mix_fnet_ffn_kernel<<<256, 512, 0, stream>>>(P);
}
```

```cpp
#include <hip/hip_runtime.h>
#include <hip/hip_bf16.h>
#include <stdint.h>

typedef __attribute__((ext_vector_type(8))) short bf16x8;
typedef __attribute__((ext_vector_type(4))) short s16x4;
typedef __attribute__((ext_vector_type(4))) float f32x4;
typedef __attribute__((ext_vector_type(2))) float f32x2;
typedef __attribute__((ext_vector_type(4))) unsigned u32x4;
typedef __attribute__((ext_vector_type(2))) unsigned u32x2;
typedef __attribute__((ext_vector_type(2))) __bf16 bf16x2_t;
#define AS3 __attribute__((address_space(3)))
#define AS1 __attribute__((address_space(1)))
typedef __amdgpu_buffer_rsrc_t rsrc_t;

namespace {
constexpr int S = 2048, D = 576, F = 4608;
constexpr int WS_EPOCH = 0;
constexpr int WS_FA = 4096;
constexpr int WS_FH = 8192;
constexpr int WS_FP = 12288;
constexpr int WS_G = 32768;
constexpr int WS_PROF = 64 << 20;
constexpr int WS_AB = 1 << 20;
constexpr int WS_XP = 6 << 20;
constexpr int WS_H = 24 << 20;
constexpr int WS_END = WS_H + 128 * 144 * 1024;
constexpr int HS = 26624;
constexpr int BS = 53248;
constexpr int LDS_PF = 3 * BS;
constexpr int LDS_GW = LDS_PF + 1024;
constexpr int LDS_G = LDS_GW + 2048;
constexpr int LDS_BYTES = LDS_G + 1024;
constexpr int BIMG = 18432;
constexpr int CODE_PF_BYTES = 26624;
constexpr unsigned SPIN_MAX = 1u << 22;
constexpr int AUX_SC1 = 16;
constexpr unsigned POISON = 0xffffffffu;

#define POLL_SLEEP 8
#define POLL_FIRST 16
#define WS_TAIL 0
#define AUX_WS 2
#define AUX_W1 0
#define AUX_W2 0
#define PF_X 1
#define PF_W1 1
#define PF_W2 1
#define PF_CODE 1
#define W1_0 2
#define W1_S 1
#define WS_0 12
#define WS_S 1
#define W2_0 14
#define W2_S 2
#define PF_WSN 0

struct Params {
  const void* x; const void* wsr; const void* wsi; const void* wdr; const void* wdi;
  const void* w1; const void* w2; const void* gw; void* out; void* ws;
};

__device__ __forceinline__ rsrc_t mk(const void* p, int bytes) {
  return __builtin_amdgcn_make_buffer_rsrc((void*)p, (short)0, bytes, 0x00020000);
}
__device__ __forceinline__ unsigned pk(float a, float b) {
  f32x2 v = {a, b};
  return __builtin_bit_cast(unsigned, __builtin_convertvector(v, bf16x2_t));
}
__device__ __forceinline__ float blo(unsigned u) { return __uint_as_float(u << 16); }
__device__ __forceinline__ float bhi(unsigned u) { return __uint_as_float(u & 0xffff0000u); }
__device__ __forceinline__ float rbf(float a) { return blo(pk(a, 0.f)); }
__device__ __forceinline__ bf16x8 ld128(AS3 char* p) { return *(AS3 bf16x8*)p; }
__device__ __forceinline__ bf16x8 ldtr(AS3 char* p) {
  s16x4 lo = __builtin_amdgcn_ds_read_tr16_b64_v4i16((AS3 s16x4*)p);
  s16x4 hi = __builtin_amdgcn_ds_read_tr16_b64_v4i16((AS3 s16x4*)(p + 4608));
  return __builtin_shufflevector(lo, hi, 0, 1, 2, 3, 4, 5, 6, 7);
}
#define MFMA(a, b, c) __builtin_amdgcn_mfma_f32_16x16x32_bf16(a, b, c, 0, 0, 0)
#define MFMA_ACC(c, a, b) asm volatile("v_mfma_f32_16x16x32_bf16 %0, %1, %2, %0" : "+v"(c) : "v"(a), "v"(b))
#define MFMA_NEW(c, a, b) asm volatile("v_mfma_f32_16x16x32_bf16 %0, %1, %2, 0" : "=&v"(c) : "v"(a), "v"(b))
#define MFMA_Z(c, a, b) do { if (Z) MFMA_NEW(c, a, b); else MFMA_ACC(c, a, b); } while (0)
#define NOP_MFMA_READ() do { SCHED(); asm volatile("s_nop 7\n\ts_nop 3"); SCHED(); } while (0)
#define NOP_VALU_WRITE() do { SCHED(); asm volatile("s_nop 3"); SCHED(); } while (0)
#define BAR() do { asm volatile("" ::: "memory"); __builtin_amdgcn_s_barrier(); asm volatile("" ::: "memory"); } while (0)
#define SCHED() __builtin_amdgcn_sched_barrier(0)
#define WAIT_LGKM0() __builtin_amdgcn_s_waitcnt(0xC07F)
template <int N> __device__ __forceinline__ void wait_vm() {
  asm volatile("s_waitcnt vmcnt(%0)" ::"n"(N) : "memory");
}
template <int AUX = 0>
__device__ __forceinline__ void dma(rsrc_t r, AS3 char* l, int voff, int soff) {
  __builtin_amdgcn_raw_ptr_buffer_load_lds(r, (AS3 void*)l, 16, voff, soff, 0, AUX);
}
template <int NT>
__device__ __forceinline__ void touch(const void* base, int off, unsigned lds_sink) {
  unsigned keep;
  if (NT)
    asm volatile("s_mov_b32 %0, m0\n\ts_mov_b32 m0, %3\n\ts_nop 0\n\tglobal_load_lds_dword %1, %2 nt\n\ts_mov_b32 m0, %0"
                 : "=&s"(keep) : "v"(off), "s"(base), "s"(lds_sink) : "memory");
  else
    asm volatile("s_mov_b32 %0, m0\n\ts_mov_b32 m0, %3\n\ts_nop 0\n\tglobal_load_lds_dword %1, %2\n\ts_mov_b32 m0, %0"
                 : "=&s"(keep) : "v"(off), "s"(base), "s"(lds_sink) : "memory");
}
template <int NT>
__device__ __forceinline__ void touch(const void* base, int, unsigned lds_sink, int off) {
  unsigned keep;
  asm volatile("s_mov_b32 %0, m0\n\ts_mov_b32 m0, %2\n\ts_nop 0\n\tglobal_load_lds_dword %1, off\n\ts_mov_b32 m0, %0"
               : "=&s"(keep) : "v"((const char*)base + off), "s"(lds_sink) : "memory");
}
__device__ __forceinline__ u32x4 swap_pair(const f32x4& a, const f32x4& b) {
  unsigned x0 = pk(a[0], a[1]), x1 = pk(a[2], a[3]), y0 = pk(b[0], b[1]), y1 = pk(b[2], b[3]);
  auto s0 = __builtin_amdgcn_permlane16_swap(x0, y0, false, false);
  auto s1 = __builtin_amdgcn_permlane16_swap(x1, y1, false, false);
  u32x4 v = {s0[0], s1[0], s0[1], s1[1]};
  return v;
}
__device__ __forceinline__ unsigned poll_issue(const AS1 unsigned* p) {
  unsigned f;
  asm volatile("global_load_dword %0, %1, off sc1" : "=v"(f) : "v"(p) : "memory");
  return f;
}
__device__ __forceinline__ void poll_check(unsigned f, const AS1 unsigned* p, unsigned tag) {
  asm volatile("" : "+v"(f));
  unsigned spins = 0;
#pragma unroll 1
  while (!__all(f == tag)) {
    __builtin_amdgcn_s_sleep(2);
    asm volatile("global_load_dword %0, %1, off sc1\n\ts_waitcnt vmcnt(0)" : "=&v"(f) : "v"(p) : "memory");
    if (++spins > SPIN_MAX) break;
  }
}
__device__ __forceinline__ void flag_store(AS1 unsigned* p, unsigned v) {
  __hip_atomic_store(p, v, __ATOMIC_RELAXED, __HIP_MEMORY_SCOPE_AGENT);
}

#define RSTAMP(i) do {} while (0)
#define STAMP(i) do {} while (0)

struct Ids { uint64_t pc0; int wg, xs, loc, rb64, part, rb, cb; };

#define LBAR() BAR()
template <int NB>
__device__ __forceinline__ void loader(const Params& P, const Ids& I, AS3 char* L, int lw, int lane, unsigned tag) {
  const int wg = I.wg, wave = lw + 4;
  (void)wg; (void)wave;
  constexpr int N1 = 2 + NB, N2 = 8 + NB;
  const rsrc_t rx = mk(P.x, S * D * 2);
  const rsrc_t rwd = mk(I.part ? P.wdi : P.wdr, D * D * 2);
  const rsrc_t rw1 = mk(P.w1, D * F * 2);
  const rsrc_t rw2 = mk(P.w2, F * D * 2);
  const rsrc_t rs = mk(I.part ? P.wsi : P.wsr, S * S * 2);
  const rsrc_t rws = mk(P.ws, WS_END);
  const int sw = ((lane & 7) ^ (((lw & 1) << 2) + (lane >> 4))) << 4;
  const int vx = (lw * 8 + (lane >> 3)) * (D * 2) + sw;
  const int vs = (lw * 8 + (lane >> 3)) * (S * 2) + sw;
  const int vl = lane * 16;
  int v1[NB], v2[NB];
#pragma unroll
  for (int i = 0; i < NB; ++i) {
    int c = (lw + 4 * i) * 64 + lane, r = c / 18, col = c - r * 18;
    int k = (r & 32) + ((r >> 2) & 3) * 8 + ((r >> 4) & 1) * 4 + (r & 3);
    v1[i] = k * (F * 2) + col * 16;
    v2[i] = k * (D * 2) + col * 16;
  }
  auto issueA = [&](int st) {
    AS3 char* b = L + (st % 6) * HS + lw * 1024;
#pragma unroll
    for (int i = 0; i < 2; ++i) dma(rx, b + i * 4096, vx, (I.rb64 * 64 + i * 32) * (D * 2) + st * 128);
#pragma unroll
    for (int i = 0; i < NB; ++i) dma(rwd, b + 8192 + i * 4096, vx, (I.cb * 144 + i * 32) * (D * 2) + st * 128);
  };
  auto issueBw = [&](int st, int sl) {
    AS3 char* b = L + sl + lw * 1024;
#pragma unroll
    for (int i = 0; i < NB; ++i) dma<AUX_W1>(rw1, b + 32768 + i * 4096, v1[i], st * 64 * (F * 2) + (I.xs * 4 + I.cb) * 288);
  };
  auto issueBx = [&](int st, int sl) {
    AS3 char* b = L + sl + lw * 1024;
#pragma unroll
    for (int i = 0; i < 8; ++i) dma(rx, b + i * 4096, vx, (I.rb * 256 + i * 32) * (D * 2) + st * 128);
  };
  auto issueC = [&](int t, int sl) {
    AS3 char* b = L + sl + lw * 1024;
#pragma unroll
    for (int i = 0; i < 8; ++i) {
      const int so = (I.rb * 256 + i * 32) * (S * 2) + (I.xs & 3) * 1024 + t * 128;
      if (t < PF_WSN) dma<0>(rs, b + i * 4096, vs, so);
      else dma<AUX_WS>(rs, b + i * 4096, vs, so);
    }
#pragma unroll
    for (int i = 0; i < NB; ++i) dma<AUX_SC1>(rws, b + 32768 + i * 4096, vl, WS_AB + ((I.xs * 8 + t) * 4 + I.cb) * BIMG + (lw + 4 * i) * 1024);
  };
  auto issueD = [&](int t, int sl) {
    AS3 char* b = L + sl + lw * 1024;
#pragma unroll
    for (int i = 0; i < NB; ++i) dma<AUX_W2>(rw2, b + 32768 + i * 4096, v2[i], (I.xs * 576 + t * 64) * (D * 2) + I.cb * 288);
#pragma unroll
    for (int i = 0; i < 8; ++i)
      dma<AUX_SC1>(rws, b + i * 4096, vl, WS_H + ((I.rb * 16 + 2 * i + (lw >> 1)) * 144 + I.xs * 18 + 2 * t + (lw & 1)) * 1024);
  };
  AS1 unsigned* wsu = (AS1 unsigned*)P.ws;
  const unsigned sink = (unsigned)(uintptr_t)(L + LDS_PF) + lw * 256;
  auto pf = [&](int step) {
    if (NB == 4) {
      if (lw == 2) {
        const int k1 = (step - W1_0) / W1_S, k2 = (step - W2_0) / W2_S, l = min(lane, 53), rr = l / 9;
        if (PF_W1 && step >= W1_0 && k1 * W1_S == step - W1_0 && k1 < 3)
          touch<0>(P.w1, (k1 * 192 + I.loc * 6 + rr) * (F * 2) + I.xs * 1152 + (l - rr * 9) * 128, sink);
        if (PF_W2 && step >= W2_0 && k2 * W2_S == step - W2_0 && k2 < 3)
          touch<0>(P.w2, (I.xs * 576 + k2 * 192) * (D * 2) + (I.loc * 54 + l) * 128, sink);
      } else {
        const int k = (step - WS_0) / WS_S;
        if (step >= WS_0 && k * WS_S == step - WS_0 && k < PF_WSN)
          touch<0>(I.part ? P.wsi : P.wsr, (I.loc * 64 + lane) * (S * 2) + (I.xs & 3) * 1024 + k * 128, sink);
      }
    }
  };
  auto arm = [&](int i) {
    const u32x4 pz = {POISON, POISON, POISON, POISON};
    __builtin_amdgcn_raw_buffer_store_b128(pz, rws, WS_XP + I.wg * 64512 + min(lw * 16 + i, 62) * 1024 + vl, 0, AUX_SC1);
  };
  STAMP(0); RSTAMP(13);
  if (lw >= 2) dma(mk(P.gw, D * 2), L + LDS_GW + (lw - 2) * 1024, vl + (lw - 2) * 1024, 0);
  int bi = 0; (void)bi;
#pragma unroll 1
  for (int st = 0; st < 6; ++st) issueA(st);
  asm volatile("" ::"s"(tag));
  wait_vm<5 * N1>(); LBAR(); pf(0);
  STAMP(1);
  wait_vm<4 * N1>(); LBAR(); issueA(6); pf(1);
  wait_vm<4 * N1>(); LBAR(); issueA(7); pf(2);
  wait_vm<4 * N1>(); LBAR(); issueA(8); pf(3);
  wait_vm<4 * N1>(); LBAR(); pf(4);
  wait_vm<3 * N1>(); LBAR(); pf(5);
  wait_vm<2 * N1>(); LBAR(); issueBw(0, 2 * BS); pf(6);
  wait_vm<N1 + NB>(); LBAR(); issueBx(0, 2 * BS); pf(7);
  wait_vm<N2>(); LBAR(); issueBw(1, 0); pf(8);
  LBAR(); issueBx(1, 0); pf(9);
  STAMP(2);
  wait_vm<N2>(); LBAR(); issueBw(2, BS); issueBx(2, BS); pf(10);
  const AS1 unsigned* pfa = wsu + WS_FA / 4 + I.cb * 64 + I.xs * 8 + (lane & 7);
  const AS1 unsigned* pfh = wsu + WS_FH / 4 + I.xs * 32 + I.rb * 4 + (lane & 3);
  const AS1 unsigned* pfp = wsu + WS_FP / 4 + I.loc * 8 + (lane & 7);
  const AS1 unsigned long long* pg = (const AS1 unsigned long long*)(wsu + WS_G / 4) + I.rb * 256 + lw * 64 + lane;
  unsigned f = 0;
  u32x2 gg = {0u, 0u};
  int sl = 2 * BS;
#pragma unroll 1
  for (int s = 1; s < 7; ++s) {
    if (s < 3) wait_vm<N2>(); else wait_vm<N2 + 1>();
    LBAR();
    if (s == 3 && lw == 0 && lane == 0) flag_store(wsu + WS_FA / 4 + I.cb * 64 + I.xs * 8 + (I.loc >> 2), tag);
    if (s == 6) {
      f = poll_issue(pfa);
      asm volatile("global_load_dwordx2 %0, %1, off sc1" : "=v"(gg) : "v"(pg) : "memory");
    }
    issueBw(s + 2, sl); issueBx(s + 2, sl); pf(10 + s);
    if (s > 1) arm(s - 2);
    sl = (sl == 2 * BS) ? 0 : sl + BS;
  }
  wait_vm<N2 + 1>(); LBAR();
  poll_check(f, pfa, tag);
  issueC(0, 2 * BS); pf(17); arm(5);
  {
    asm volatile("" : "+v"(gg));
    unsigned spins = 0;
#pragma unroll 1
    while (!__all(gg[1] == tag)) {
      __builtin_amdgcn_s_sleep(2);
      asm volatile("global_load_dwordx2 %0, %1, off sc1\n\ts_waitcnt vmcnt(0)" : "=&v"(gg) : "v"(pg) : "memory");
      if (++spins > SPIN_MAX) break;
    }
    asm volatile("ds_write_b32 %0, %1\n\ts_waitcnt lgkmcnt(0)" ::"v"((unsigned)(uintptr_t)(L + LDS_G + (lw * 64 + lane) * 4)), "v"(gg[0]) : "memory");
  }
  wait_vm<N2 + 2>(); LBAR(); issueC(1, 0); pf(18); arm(6);
  LBAR(); issueC(2, BS); pf(19); arm(7);
  STAMP(3);
  wait_vm<2 * N2 + 3>(); LBAR();
  sl = 2 * BS;
#pragma unroll 1
  for (int s = 10; s < 15; ++s) {
    wait_vm<N2 + 2>(); LBAR();
    if (s == 13 && lw == 0 && lane == 0) flag_store(wsu + WS_FH / 4 + I.xs * 32 + I.loc, tag);
    if (s == 14) f = poll_issue(pfh);
    issueC(s - 7, sl); pf(10 + s); arm(s - 2);
    sl = (sl == 2 * BS) ? 0 : sl + BS;
  }
  wait_vm<N2 + 1>(); LBAR();
  STAMP(4);
  poll_check(f, pfh, tag);
  STAMP(5);
  issueD(0, BS); pf(25); arm(13);
  sl = 2 * BS;
#pragma unroll 1
  for (int s = 16; s < 24; ++s) {
    if (s < 19) wait_vm<N2 + 2>(); else wait_vm<N2>();
    LBAR();
    if (s == 20 && lw == 0 && lane == 0) flag_store(wsu + WS_FP / 4 + I.loc * 8 + I.xs, tag);
    if (s == 23) f = poll_issue(pfp);
    issueD(s - 15, sl); pf(10 + s);
    if (s < 18) arm(s - 2);
    sl = (sl == 2 * BS) ? 0 : sl + BS;
  }
  wait_vm<N2>(); LBAR();
  poll_check(f, pfp, tag);
  wait_vm<0>(); LBAR();
  LBAR();
  STAMP(6);
  if (I.wg == 0 && lw == 1) {
    const AS1 unsigned* pa = wsu + WS_FA / 4 + lane;
#pragma unroll 1
    for (int i = 0; i < 4; ++i) poll_check(0u - (tag == 0u), pa + i * 64, tag);
    if (lane == 0) flag_store(wsu + WS_EPOCH / 4, tag);
  }
  BAR();
  STAMP(7);
}

template <bool Z = false>
__device__ __forceinline__ void unit(f32x4 (&acc)[4][9], const bf16x8 (&Ac)[4], bf16x8 (&An)[4], bf16x8 (&B)[7],
                                     const bf16x8 (&Xc)[2], bf16x8 (&Xn)[2], AS3 char* a, AS3 char* b) {
  SCHED();
#pragma unroll
  for (int n = 0; n < 7; ++n) {
    MFMA_Z(acc[0][n], B[n], Ac[0]);
    SCHED();
    if (n < 4) An[n] = ld128(a + n * 2048);
    SCHED();
    MFMA_Z(acc[1][n], B[n], Ac[1]);
    SCHED();
    if (n < 2) Xn[n] = ldtr(b + (7 + n) * 32);
    SCHED();
    MFMA_Z(acc[2][n], B[n], Ac[2]);
    MFMA_Z(acc[3][n], B[n], Ac[3]);
    SCHED();
    B[n] = ldtr(b + n * 32);
    SCHED();
  }
#pragma unroll
  for (int n = 0; n < 2; ++n)
#pragma unroll
    for (int m = 0; m < 4; ++m) MFMA_Z(acc[m][7 + n], Xc[n], Ac[m]);
  SCHED();
  WAIT_LGKM0();
}
__device__ __forceinline__ void first_read(bf16x8 (&A)[4], bf16x8 (&B)[7], bf16x8 (&X)[2], AS3 char* a, AS3 char* b) {
#pragma unroll
  for (int m = 0; m < 4; ++m) A[m] = ld128(a + m * 2048);
#pragma unroll
  for (int n = 0; n < 7; ++n) B[n] = ldtr(b + n * 32);
#pragma unroll
  for (int n = 0; n < 2; ++n) X[n] = ldtr(b + (7 + n) * 32);
  WAIT_LGKM0();
}
template <int ODD>
__device__ __forceinline__ void store_h(const f32x4 (&acc)[4][9], const float (&om)[4], rsrc_t rws, int sbase, int r, int q) {
  const int vp = ((((q & 1) << 1) | (q >> 1)) * 16 + r) * 16 + sbase + ODD * 1024;
  const int vh = ((ODD * 2 + (q >> 1)) * 16 + r) * 16 + (q & 1) * 8 + sbase + (ODD ? 0 : 4096);
#pragma unroll
  for (int m = 0; m < 4; ++m) {
    f32x4 t[9];
#pragma unroll
    for (int n = 0; n < 9; ++n)
#pragma unroll
      for (int e = 0; e < 4; ++e) t[n][e] = fmaxf(acc[m][n][e], 0.f) * om[m];
#pragma unroll
    for (int p = 0; p < 4; ++p)
      __builtin_amdgcn_raw_buffer_store_b128(swap_pair(t[2 * p + ODD], t[2 * p + 1 + ODD]), rws,
                                             vp + m * 147456 + p * 1024, 0, AUX_SC1);
    const f32x4 s = t[ODD ? 0 : 8];
    u32x2 v = {pk(s[0], s[1]), pk(s[2], s[3])};
    __builtin_amdgcn_raw_buffer_store_b64(v, rws, vh + m * 147456, 0, AUX_SC1);
  }
}

__device__ __forceinline__ void consumer(const Params& P, const Ids& I, AS3 char* L, int cw, int lane, unsigned tag) {
  const int wg = I.wg, wave = cw;
  (void)wg; (void)wave;
  const int r = lane & 15, q = lane >> 4;
  const rsrc_t rws = mk(P.ws, WS_END);
  STAMP(0);
  asm volatile("" ::"s"(tag));
  const unsigned sink = (unsigned)(uintptr_t)(L + LDS_PF) + cw * 256;
  if (PF_X && cw == 0) touch<0>(P.x, (I.wg * 72 + lane) * 128, sink);
  if (PF_X && cw == 1)
    touch<0>(lane < 8 ? P.x : (I.wg < 128 ? P.wdr : P.wdi), 0, sink, lane < 8 ? (I.wg * 72 + 64 + lane) * 128 : min((I.wg & 127) * 41 + lane - 8, 5183) * 128);
  if (PF_CODE && cw >= 2) touch<0>((const void*)I.pc0, min(((I.loc & 1) * 128 + (cw - 2) * 64 + lane) * 128, CODE_PF_BYTES), sink);
  const int aL0 = r * 128 + ((q ^ ((r >> 1) & 7)) << 4), aL1 = aL0 ^ 64;
  const int bL = (4 * q + ((lane >> 2) & 3)) * 288 + (lane & 3) * 8;
  f32x4 acc[4][9];
  {
    f32x4 lg = {0.f, 0.f, 0.f, 0.f};
#pragma unroll
    for (int n = 0; n < 9; ++n) { acc[0][n] = f32x4{0.f, 0.f, 0.f, 0.f}; asm volatile("" : "+v"(acc[0][n])); }
    asm volatile("" : "+v"(lg));
    bf16x8 xa, xb, w0[10], w1[10];
    BAR();
    STAMP(1);
    xa = ld128(L + aL0 + cw * 2048); xb = ld128(L + aL1 + cw * 2048);
#pragma unroll
    for (int n = 0; n < 9; ++n) { w0[n] = ld128(L + aL0 + 8192 + n * 2048); w1[n] = ld128(L + aL1 + 8192 + n * 2048); }
    w0[9] = ld128(L + LDS_GW + q * 16); w1[9] = ld128(L + LDS_GW + q * 16 + 64);
    WAIT_LGKM0();
    NOP_VALU_WRITE();
    int hs = HS;
#pragma unroll 1
    for (int st = 0; st < 9; ++st) {
      BAR();
      AS3 char* b0 = L + hs + aL0;
      AS3 char* b1 = L + hs + aL1;
      AS3 char* bg = L + LDS_GW + (st + 1) * 128 + q * 16;
      SCHED();
#pragma unroll
      for (int n = 0; n < 9; ++n) { MFMA_ACC(acc[0][n], w0[n], xa); SCHED(); w0[n] = ld128(b0 + 8192 + n * 2048); SCHED(); }
      MFMA_ACC(lg, w0[9], xa); SCHED(); w0[9] = ld128(bg); xa = ld128(b0 + cw * 2048); SCHED();
#pragma unroll
      for (int n = 0; n < 9; ++n) { MFMA_ACC(acc[0][n], w1[n], xb); SCHED(); w1[n] = ld128(b1 + 8192 + n * 2048); SCHED(); }
      MFMA_ACC(lg, w1[9], xb); SCHED(); w1[9] = ld128(bg + 64); xb = ld128(b1 + cw * 2048); SCHED();
      WAIT_LGKM0();
      hs = (st == 4) ? 0 : hs + HS;
    }
    NOP_MFMA_READ();
    STAMP(2);
    if (q == 0 && (r >> 1) == I.part * 4 + I.cb) {
      const float g = rbf(1.f / (1.f + __expf(-rbf(lg[0]))));
      __hip_atomic_store((AS1 unsigned long long*)((AS1 char*)P.ws + WS_G) + I.rb64 * 64 + cw * 16 + r,
                         ((unsigned long long)tag << 32) | __float_as_uint(g), __ATOMIC_RELAXED, __HIP_MEMORY_SCOPE_AGENT);
    }
    const float sg = I.part ? -1.f : 1.f;
    const int sl = cw * 16 + r, k = sl & 31;
    const int rho = (sl & 32) + ((k >> 2) & 1) * 16 + (k >> 3) * 4 + (k & 3);
    const int sb = WS_AB + ((I.part * 32 + I.rb64) * 4 + I.cb) * BIMG;
    const int vp = sb + rho * 288 + (((q & 1) << 1) | (q >> 1)) * 16;
#pragma unroll
    for (int p = 0; p < 4; ++p)
      __builtin_amdgcn_raw_buffer_store_b128(swap_pair(acc[0][2 * p] * sg, acc[0][2 * p + 1] * sg), rws, vp + p * 64, 0, AUX_SC1);
    const f32x4 s = acc[0][8] * sg;
    u32x2 v = {pk(s[0], s[1]), pk(s[2], s[3])};
    __builtin_amdgcn_raw_buffer_store_b64(v, rws, sb + rho * 288 + 256 + q * 8, 0, AUX_SC1);
  }
  bf16x8 A0[4], A1[4], B[7], X0[2], X1[2];
  const int a0 = aL0 + cw * 8192, a1 = aL1 + cw * 8192, b0 = bL + 32768;
  int sl = 2 * BS;
#define NEXT_SLOT() sl = (sl == 2 * BS) ? 0 : sl + BS
  {
#pragma unroll
    for (int m = 0; m < 4; ++m)
#pragma unroll
      for (int n = 0; n < 9; ++n) { acc[m][n] = f32x4{0.f, 0.f, 0.f, 0.f}; asm volatile("" : "+v"(acc[m][n])); }
    BAR();
    STAMP(3);
    first_read(A0, B, X0, L + sl + a0, L + sl + b0);
    NOP_VALU_WRITE();
#pragma unroll 1
    for (int st = 0; st < 9; ++st) {
      if (st == 2) wait_vm<0>();
      unit(acc, A0, A1, B, X0, X1, L + sl + a1, L + sl + b0 + 9216);
      BAR();
      NEXT_SLOT();
      unit(acc, A1, A0, B, X1, X0, L + sl + a0, L + sl + b0);
    }
    NOP_MFMA_READ();
    STAMP(4);
    float om[4];
#pragma unroll
    for (int m = 0; m < 4; ++m) {
      om[m] = rbf(1.f - *(AS3 float*)(L + LDS_G + (cw * 64 + m * 16 + r) * 4));
    }
    const int sb = WS_H + ((I.rb * 16 + cw * 4) * 144 + I.xs * 18 + ((I.cb * 9) >> 1)) * 1024;
    if (I.cb & 1) store_h<1>(acc, om, rws, sb, r, q);
    else store_h<0>(acc, om, rws, sb, r, q);
    STAMP(5);
  }
  {
    asm volatile("" : "+v"(lane));
    const int h0 = cw * 8192 + lane * 16;
    BAR();
    STAMP(6);
    first_read(A0, B, X0, L + sl + a0, L + sl + b0);
    unit<true>(acc, A0, A1, B, X0, X1, L + sl + a1, L + sl + b0 + 9216);
    BAR();
    NEXT_SLOT();
    unit(acc, A1, A0, B, X1, X0, L + sl + a0, L + sl + b0);
#pragma unroll 1
    for (int st = 10; st < 17; ++st) {
      if (st == 12) wait_vm<0>();
      unit(acc, A0, A1, B, X0, X1, L + sl + a1, L + sl + b0 + 9216);
      BAR();
      NEXT_SLOT();
      unit(acc, A1, A0, B, X1, X0, L + sl + (st < 16 ? a0 : h0), L + sl + b0);
    }
    NOP_MFMA_READ();
    STAMP(7);
#pragma unroll
    for (int m = 0; m < 4; ++m) {
      const float g = *(AS3 float*)(L + LDS_G + (cw * 64 + m * 16 + (lane & 15)) * 4);
#pragma unroll
      for (int n = 0; n < 9; ++n) { acc[m][n] *= g; asm volatile("" : "+v"(acc[m][n])); }
    }
    NOP_VALU_WRITE();
#pragma unroll 1
    for (int st = 17; st < 26; ++st) {
      unit(acc, A0, A1, B, X0, X1, L + sl + h0 + 1024, L + sl + b0 + 9216);
      BAR();
      NEXT_SLOT();
      unit(acc, A1, A0, B, X1, X0, L + sl + h0, L + sl + b0);
    }
    NOP_MFMA_READ();
    STAMP(8);
  }
  asm volatile("" : "+v"(lane));
#pragma unroll
  for (int hf = 0; hf < 2; ++hf) {
    const int dst = cw * 2 + hf;
    if (dst == I.xs) {
      AS3 char* kp = L + lane * 16;
#pragma unroll
      for (int m = 0; m < 2; ++m) {
#pragma unroll
        for (int n = 0; n < 8; ++n) *(AS3 f32x4*)(kp + (m * 8 + n) * 1024) = acc[2 * hf + m][n];
        *(AS3 f32x4*)(kp + (16 + m) * 1024) = acc[2 * hf + m][8];
      }
    } else {
      const int sb = WS_XP + ((I.loc * 8 + dst) * 7 + ((I.xs - dst - 1) & 7)) * 9216 + lane * 16;
#pragma unroll
      for (int it = 0; it < 9; ++it) {
        const f32x4 a = it < 8 ? acc[2 * hf + (it >> 2)][(it & 3) * 2] : acc[2 * hf][8];
        const f32x4 b = it < 8 ? acc[2 * hf + (it >> 2)][(it & 3) * 2 + 1] : acc[2 * hf + 1][8];
        u32x4 v = {min(pk(a[0], a[1]), POISON - 1), min(pk(a[2], a[3]), POISON - 1), min(pk(b[0], b[1]), POISON - 1), min(pk(b[2], b[3]), POISON - 1)};
        __builtin_amdgcn_raw_buffer_store_b128(v, rws, sb + it * 1024, 0, AUX_SC1);
      }
    }
  }
  WAIT_LGKM0();
  STAMP(9);
  BAR();
}

__device__ __forceinline__ void final_items(const Params& P, const Ids& I, AS3 char* L, int w, int lane) {
  const rsrc_t rws = mk(P.ws, WS_END);
  const rsrc_t ro = mk(P.out, S * D * 2);
  const int r = lane & 15, q = lane >> 4;
  const int vb = WS_XP + I.wg * 64512 + lane * 16;
  f32x4 a = *(AS3 f32x4*)(L + w * 2048 + lane * 16);
  f32x4 b = *(AS3 f32x4*)(L + w * 2048 + 1024 + lane * 16);
  f32x4 c = *(AS3 f32x4*)(L + 16384 + (w & 1) * 1024 + lane * 16);
  u32x4 p[7];
  u32x2 e[7];
#pragma unroll
  for (int i = 0; i < 7; ++i) e[i] = u32x2{0u, 0u};
  unsigned todo = 0x7f;
#pragma unroll 1
  for (unsigned spins = 0; todo && spins < SPIN_MAX; ++spins) {
#pragma unroll
    for (int i = 0; i < 7; ++i)
      if (todo >> i & 1) {
        p[i] = __builtin_amdgcn_raw_buffer_load_b128(rws, vb + i * 9216 + w * 1024, 0, AUX_SC1);
        if (w < 2) e[i] = __builtin_amdgcn_raw_buffer_load_b64(rws, vb + i * 9216 + 8192 + w * 8, 0, AUX_SC1);
      }
#pragma unroll
    for (int i = 0; i < 7; ++i)
      if (todo >> i & 1) {
        const unsigned mx = max(max(max(p[i][0], p[i][1]), max(p[i][2], p[i][3])), max(e[i][0], e[i][1]));
        if (__all(mx != POISON)) {
          todo &= ~(1u << i);
          a += f32x4{blo(p[i][0]), bhi(p[i][0]), blo(p[i][1]), bhi(p[i][1])};
          b += f32x4{blo(p[i][2]), bhi(p[i][2]), blo(p[i][3]), bhi(p[i][3])};
          c += f32x4{blo(e[i][0]), bhi(e[i][0]), blo(e[i][1]), bhi(e[i][1])};
        }
      }
    if (todo) __builtin_amdgcn_s_sleep(POLL_SLEEP);
  }
  const int row0 = I.rb * 256 + I.xs * 32, col0 = I.cb * 144;
  __builtin_amdgcn_raw_buffer_store_b128(swap_pair(a, b), ro, (row0 + (w >> 2) * 16 + r) * (D * 2) + (col0 + (w & 3) * 32) * 2 + (((q & 1) << 1) | (q >> 1)) * 16, 0, 0);
  if (w < 2) {
    u32x2 v = {pk(c[0], c[1]), pk(c[2], c[3])};
    __builtin_amdgcn_raw_buffer_store_b64(v, ro, (row0 + w * 16 + r) * (D * 2) + (col0 + 128) * 2 + q * 8, 0, 0);
  }
}
}

__global__ __launch_bounds__(512, 2) void gated_mix_fnet_ffn_kernel(Params P) {
  __shared__ __attribute__((aligned(1024))) char lds[LDS_BYTES];
  AS3 char* L = (AS3 char*)lds;
  const int lane = threadIdx.x & 63;
  const int wave = __builtin_amdgcn_readfirstlane(threadIdx.x >> 6);
  Ids I;
  asm volatile("s_getpc_b64 %0" : "=s"(I.pc0));
  I.wg = blockIdx.x;
  I.xs = I.wg & 7; I.loc = I.wg >> 3;
  I.part = I.xs >> 2; I.rb = I.loc >> 2; I.cb = I.loc & 3;
  I.rb64 = (I.xs & 3) * 8 + I.rb;
  const unsigned tag = *(const __attribute__((address_space(4))) unsigned*)((uintptr_t)P.ws + WS_EPOCH) + 1u;
  if (wave < 4) consumer(P, I, L, wave, lane, tag);
  else if (wave < 6) loader<5>(P, I, L, wave - 4, lane, tag);
  else loader<4>(P, I, L, wave - 4, lane, tag);
  {
    int tz = threadIdx.x;
    asm volatile("" : "+v"(tz));
    const int w2 = __builtin_amdgcn_readfirstlane(tz >> 6);
    __builtin_amdgcn_s_sleep(POLL_FIRST);
    final_items(P, I, L, w2, tz & 63);
  }
}

extern "C" void kernel_launch(void* const* d_in, const int* in_sizes, int n_in, void* d_out, int out_size, void* d_ws,
                              size_t ws_size, hipStream_t stream) {
  Params P;
  P.x = d_in[0]; P.wsr = d_in[1]; P.wsi = d_in[2]; P.wdr = d_in[3]; P.wdi = d_in[4];
  P.w1 = d_in[6]; P.w2 = d_in[7]; P.gw = d_in[8]; P.out = d_out;
  P.ws = WS_TAIL ? (char*)d_ws + ((ws_size - WS_END) & ~(size_t)0xfffff) : (char*)d_ws;
  gated_mix_fnet_ffn_kernel<<<256, 512, 0, stream>>>(P);
}
```

```cpp
#include <hip/hip_runtime.h>
#include <hip/hip_bf16.h>
#include <stdint.h>

typedef __attribute__((ext_vector_type(8))) short bf16x8;
typedef __attribute__((ext_vector_type(4))) short s16x4;
typedef __attribute__((ext_vector_type(4))) float f32x4;
typedef __attribute__((ext_vector_type(2))) float f32x2;
typedef __attribute__((ext_vector_type(4))) unsigned u32x4;
typedef __attribute__((ext_vector_type(2))) unsigned u32x2;
typedef __attribute__((ext_vector_type(2))) __bf16 bf16x2_t;
#define AS3 __attribute__((address_space(3)))
#define AS1 __attribute__((address_space(1)))
typedef __amdgpu_buffer_rsrc_t rsrc_t;

namespace {
constexpr int S = 2048, D = 576, F = 4608;
constexpr int WS_EPOCH = 0;
constexpr int WS_FA = 4096;
constexpr int WS_FH = 8192;
constexpr int WS_G = 32768;
constexpr int WS_PROF = 64 << 20;
constexpr int WS_AB = 1 << 20;
constexpr int WS_XP = 6 << 20;
constexpr int WS_H = 16 << 20;
constexpr int WS_END = WS_H + 128 * 144 * 1024;
constexpr int HS = 26624;
constexpr int BS = 53248;
constexpr int LDS_PF = 3 * BS;
constexpr int LDS_GW = LDS_PF + 1024;
constexpr int LDS_G = LDS_GW + 2048;
constexpr int LDS_BYTES = LDS_G + 1024;
constexpr int BIMG = 18432;
constexpr int CODE_PF_BYTES = 26624;
constexpr unsigned SPIN_MAX = 1u << 22;
constexpr int AUX_SC1 = 16;
constexpr unsigned POISON = 0xffffffffu;

#define POLL_SLEEP 4
#define POLL_FIRST 8
#define PF_X 0
#define ROT_A 1
#define ROT_B 1
#define ROT_C 1
#define ROT_D 2
#define AUX_WS 0
#define AUX_W1 0
#define AUX_W2 0
#define AUX_WD 0
#define PF_WD 1
#define PF_W1 0
#define PF_WS 1
#define PF_W2 0
#define NT_W1 0
#define NT_WS 0
#define NT_W2 0
#define W1_0 3
#define W1_S 1
#define WS_0 12
#define WS_S 1
#define W2_0 23
#define W2_S 2
#define PF_WSN 8
#define PF_CODE 1

struct Params {
  const void* x; const void* wsr; const void* wsi; const void* wdr; const void* wdi;
  const void* w1; const void* w2; const void* gw; void* out; void* ws;
};

__device__ __forceinline__ rsrc_t mk(const void* p, int bytes) {
  return __builtin_amdgcn_make_buffer_rsrc((void*)p, (short)0, bytes, 0x00020000);
}
__device__ __forceinline__ int cslot(int s) {
  int i = s & 3;
  return i == 0 ? 124928 : (i == 1 ? 69632 : (i == 2 ? 0 : 34816));
}
__device__ __forceinline__ unsigned pk(float a, float b) {
  f32x2 v = {a, b};
  return __builtin_bit_cast(unsigned, __builtin_convertvector(v, bf16x2_t));
}
__device__ __forceinline__ float blo(unsigned u) { return __uint_as_float(u << 16); }
__device__ __forceinline__ float bhi(unsigned u) { return __uint_as_float(u & 0xffff0000u); }
__device__ __forceinline__ float rbf(float a) { return blo(pk(a, 0.f)); }
__device__ __forceinline__ bf16x8 ld128(AS3 char* p) { return *(AS3 bf16x8*)p; }
__device__ __forceinline__ bf16x8 ldtr(AS3 char* p) {
  s16x4 lo = __builtin_amdgcn_ds_read_tr16_b64_v4i16((AS3 s16x4*)p);
  s16x4 hi = __builtin_amdgcn_ds_read_tr16_b64_v4i16((AS3 s16x4*)(p + 4608));
  return __builtin_shufflevector(lo, hi, 0, 1, 2, 3, 4, 5, 6, 7);
}
#define MFMA(a, b, c) __builtin_amdgcn_mfma_f32_16x16x32_bf16(a, b, c, 0, 0, 0)
#define MFMA_ACC(c, a, b) asm volatile("v_mfma_f32_16x16x32_bf16 %0, %1, %2, %0" : "+v"(c) : "v"(a), "v"(b))
#define MFMA_NEW(c, a, b) asm volatile("v_mfma_f32_16x16x32_bf16 %0, %1, %2, 0" : "=&v"(c) : "v"(a), "v"(b))
#define MFMA_Z(c, a, b) do { if (Z) MFMA_NEW(c, a, b); else MFMA_ACC(c, a, b); } while (0)
#define NOP_MFMA_READ() do { SCHED(); asm volatile("s_nop 7\n\ts_nop 3"); SCHED(); } while (0)
#define NOP_VALU_WRITE() do { SCHED(); asm volatile("s_nop 3"); SCHED(); } while (0)
#define BAR() do { asm volatile("" ::: "memory"); __builtin_amdgcn_s_barrier(); asm volatile("" ::: "memory"); } while (0)
#define SCHED() __builtin_amdgcn_sched_barrier(0)
#define WAIT_LGKM0() __builtin_amdgcn_s_waitcnt(0xC07F)
template <int N> __device__ __forceinline__ void wait_vm() {
  asm volatile("s_waitcnt vmcnt(%0)" ::"n"(N) : "memory");
}
template <int AUX = 0>
__device__ __forceinline__ void dma(rsrc_t r, AS3 char* l, int voff, int soff) {
  __builtin_amdgcn_raw_ptr_buffer_load_lds(r, (AS3 void*)l, 16, voff, soff, 0, AUX);
}
template <int NT>
__device__ __forceinline__ void touch(const void* base, int off, unsigned lds_sink) {
  unsigned keep;
  if (NT)
    asm volatile("s_mov_b32 %0, m0\n\ts_mov_b32 m0, %3\n\ts_nop 0\n\tglobal_load_lds_dword %1, %2 nt\n\ts_mov_b32 m0, %0"
                 : "=&s"(keep) : "v"(off), "s"(base), "s"(lds_sink) : "memory");
  else
    asm volatile("s_mov_b32 %0, m0\n\ts_mov_b32 m0, %3\n\ts_nop 0\n\tglobal_load_lds_dword %1, %2\n\ts_mov_b32 m0, %0"
                 : "=&s"(keep) : "v"(off), "s"(base), "s"(lds_sink) : "memory");
}
template <int NT>
__device__ __forceinline__ void touch(const void* base, int, unsigned lds_sink, int off) {
  unsigned keep;
  asm volatile("s_mov_b32 %0, m0\n\ts_mov_b32 m0, %2\n\ts_nop 0\n\tglobal_load_lds_dword %1, off\n\ts_mov_b32 m0, %0"
               : "=&s"(keep) : "v"((const char*)base + off), "s"(lds_sink) : "memory");
}
__device__ __forceinline__ u32x4 swap_pair(const f32x4& a, const f32x4& b) {
  unsigned x0 = pk(a[0], a[1]), x1 = pk(a[2], a[3]), y0 = pk(b[0], b[1]), y1 = pk(b[2], b[3]);
  auto s0 = __builtin_amdgcn_permlane16_swap(x0, y0, false, false);
  auto s1 = __builtin_amdgcn_permlane16_swap(x1, y1, false, false);
  u32x4 v = {s0[0], s1[0], s0[1], s1[1]};
  return v;
}
__device__ __forceinline__ unsigned poll_issue(const AS1 unsigned* p) {
  unsigned f;
  asm volatile("global_load_dword %0, %1, off sc1" : "=v"(f) : "v"(p) : "memory");
  return f;
}
__device__ __forceinline__ void poll_check(unsigned f, const AS1 unsigned* p, unsigned tag) {
  asm volatile("" : "+v"(f));
  unsigned spins = 0;
#pragma unroll 1
  while (!__all(f == tag)) {
    __builtin_amdgcn_s_sleep(2);
    asm volatile("global_load_dword %0, %1, off sc1\n\ts_waitcnt vmcnt(0)" : "=&v"(f) : "v"(p) : "memory");
    if (++spins > SPIN_MAX) break;
  }
}
__device__ __forceinline__ void flag_store(AS1 unsigned* p, unsigned v) {
  __hip_atomic_store(p, v, __ATOMIC_RELAXED, __HIP_MEMORY_SCOPE_AGENT);
}

#define RSTAMP(i) do {} while (0)
#define STAMP(i) do {} while (0)

__device__ __forceinline__ int rot9(int st, int k) { const int t = st + k; return t >= 9 ? t - 9 : t; }
__device__ __forceinline__ int rot4(int st, int k, int lim) { return st < lim ? (st & ~3) | ((st + k) & 3) : st; }
struct Ids { uint64_t pc0; int wg, rho, sig, loc, rb64, pc, rb256, j, rb128, cb, ra; };

#define LBAR() BAR()
template <int NB>
__device__ __forceinline__ void loader(const Params& P, const Ids& I, AS3 char* L, int lw, int lane, unsigned tag) {
  const int wg = I.wg, wave = lw + 4;
  (void)wg; (void)wave;
  constexpr int N1 = 2 + NB, N2 = 8 + NB, N3 = 4 + NB;
  const rsrc_t rx = mk(P.x, S * D * 2);
  const rsrc_t rwd = mk((I.pc >> 2) ? P.wdi : P.wdr, D * D * 2);
  const rsrc_t rw1 = mk(P.w1, D * F * 2);
  const rsrc_t rw2 = mk(P.w2, F * D * 2);
  const rsrc_t rs = mk((I.sig >> 1) ? P.wsi : P.wsr, S * S * 2);
  const rsrc_t rws = mk(P.ws, WS_END);
  const int sw = ((lane & 7) ^ (((lw & 1) << 2) + (lane >> 4))) << 4;
  const int vx = (lw * 8 + (lane >> 3)) * (D * 2) + sw;
  const int vs = (lw * 8 + (lane >> 3)) * (S * 2) + sw;
  const int vl = lane * 16;
  int v1[NB], v2[NB];
#pragma unroll
  for (int i = 0; i < NB; ++i) {
    int c = (lw + 4 * i) * 64 + lane, r = c / 18, col = c - r * 18;
    int k = (r & 32) + ((r >> 2) & 3) * 8 + ((r >> 4) & 1) * 4 + (r & 3);
    v1[i] = k * (F * 2) + col * 16;
    v2[i] = k * (D * 2) + col * 16;
  }
  auto issueAx = [&](int st) {
    AS3 char* b = L + (st & 3) * HS + lw * 1024;
#pragma unroll
    for (int i = 0; i < 2; ++i) dma(rx, b + i * 4096, vx, (I.rb64 * 64 + i * 32) * (D * 2) + rot9(st, I.ra) * 128);
  };
  auto issueAw = [&](int st) {
    AS3 char* b = L + (st & 3) * HS + lw * 1024;
#pragma unroll
    for (int i = 0; i < NB; ++i) dma<AUX_WD>(rwd, b + 8192 + i * 4096, vx, ((I.pc & 3) * 144 + i * 32) * (D * 2) + rot9(st, I.ra) * 128);
  };
  auto issueA = [&](int st) { issueAx(st); issueAw(st); };
  auto issueBw = [&](int st) {
    AS3 char* b = L + (2 - st % 3) * BS + lw * 1024;
#pragma unroll
    for (int i = 0; i < NB; ++i) dma<AUX_W1>(rw1, b + 32768 + i * 4096, v1[i], rot4(st, ROT_B * I.rb256, 8) * 64 * (F * 2) + (I.sig * 8 + I.j) * 288);
  };
  auto issueBx = [&](int st) {
    AS3 char* b = L + (2 - st % 3) * BS + lw * 1024;
#pragma unroll
    for (int i = 0; i < 8; ++i) dma(rx, b + i * 4096, vx, (I.rb256 * 256 + i * 32) * (D * 2) + rot4(st, ROT_B * I.rb256, 8) * 128);
  };
  auto issueB = [&](int st) { issueBw(st); issueBx(st); };
  auto issueC = [&](int st) {
    AS3 char* b = L + cslot(st) + lw * 1024;
#pragma unroll
    for (int i = 0; i < 4; ++i) {
      const int so = (I.rb128 * 128 + i * 32) * (S * 2) + (I.sig & 1) * 2048 + rot4(st, ROT_C * I.cb, 16) * 128;
      dma<AUX_WS>(rs, b + i * 4096, vs, so);
    }
#pragma unroll
    for (int i = 0; i < NB; ++i) dma<AUX_SC1>(rws, b + 16384 + i * 4096, vl, WS_AB + ((I.sig * 16 + rot4(st, ROT_C * I.cb, 16)) * 4 + I.cb) * BIMG + (lw + 4 * i) * 1024);
  };
  auto issueD = [&](int st) {
    AS3 char* b = L + cslot(st) + lw * 1024;
    const int t = ROT_D == 2 ? (st < 32 ? ((st - 16) & 8) | ((st + I.rb128) & 7) : st - 16) : rot4(st - 16, ROT_D * I.rb128, 16);
#pragma unroll
    for (int i = 0; i < NB; ++i) dma<AUX_W2>(rw2, b + 16384 + i * 4096, v2[i], (I.sig * 1152 + t * 64) * (D * 2) + I.cb * 288);
#pragma unroll
    for (int i = 0; i < 4; ++i)
      dma<AUX_SC1>(rws, b + i * 4096, vl, WS_H + ((I.rb128 * 8 + 2 * i + (lw >> 1)) * 144 + I.sig * 36 + 2 * t + (lw & 1)) * 1024);
  };
  AS1 unsigned* wsu = (AS1 unsigned*)P.ws;

  const unsigned sink = (unsigned)(uintptr_t)(L + LDS_PF) + lw * 256;
  auto pfW1 = [&](int st) {
    const int l = min(lane, 35), hi = l >= 18;
    if (PF_W1) touch<NT_W1>(P.w1, (st * 64 + I.loc * 2 + hi) * (F * 2) + I.sig * 2304 + (l - hi * 18) * 128, sink);
  };
  auto pfWs = [&](int st) {
    if (PF_WS) touch<NT_WS>((I.sig >> 1) ? P.wsi : P.wsr,
                            (I.rho * 1024 + I.loc * 32 + (lane >> 1)) * (S * 2) + (I.sig & 1) * 2048 + (st + (lane & 1)) * 128, sink);
  };
  auto pfW2 = [&](int t) {
    if (PF_W2) touch<NT_W2>(P.w2, (I.sig * 1152 + t * 64) * (D * 2) + (I.loc * 54 + min(lane, 53)) * 128, sink);
  };
  auto pf = [&](int step) {
    if (NB == 4) {
      if (lw == 2) {
        const int k1 = (step - W1_0) / W1_S, k2 = (step - W2_0) / W2_S;
        if (step >= W1_0 && k1 * W1_S == step - W1_0 && k1 < 9) pfW1(k1);
        if (step >= W2_0 && k2 * W2_S == step - W2_0 && k2 < 6) pfW2(k2 * 3);
      } else {
        const int k = (step - WS_0) / WS_S;
        if (step >= WS_0 && k * WS_S == step - WS_0 && k * 2 < PF_WSN) pfWs(k * 2);
      }
    }
  };
  auto arm = [&](int i) {
    const u32x4 pz = {POISON, POISON, POISON, POISON};
    __builtin_amdgcn_raw_buffer_store_b128(pz, rws, WS_XP + I.wg * 27648 + min(lw * 7 + i, 26) * 1024 + vl, 0, AUX_SC1);
  };
  STAMP(0); RSTAMP(13);
  if (lw >= 2) dma(mk(P.gw, D * 2), L + LDS_GW + (lw - 2) * 1024, vl + (lw - 2) * 1024, 0);
  int bi = 0; (void)bi;
  issueA(0); issueA(1); issueA(2);
  asm volatile("" ::"s"(tag));
  wait_vm<2 * N1>(); LBAR(); issueA(3); pf(0);
  STAMP(1);
#pragma unroll 1
  for (int k = 0; k < 5; ++k) { wait_vm<2 * N1>(); LBAR(); issueA(k + 4); pf(k + 1); }
  wait_vm<2 * N1>(); LBAR(); issueBw(0); pf(6);
  wait_vm<N1 + NB>(); LBAR(); issueBx(0); pf(7);
  wait_vm<N2>(); LBAR(); issueBw(1); pf(8);
  LBAR(); issueBx(1); pf(9);
  STAMP(2);
  wait_vm<N2>(); LBAR(); issueB(2); pf(10);
  const AS1 unsigned* pfa = wsu + WS_FA / 4 + I.cb * 64 + I.sig * 16 + (lane & 15);
  const AS1 unsigned long long* pg = (const AS1 unsigned long long*)(wsu + WS_G / 4) + I.rb256 * 256 + lw * 64 + lane;
  unsigned f = 0;
  u32x2 gg = {0u, 0u};
#pragma unroll 1
  for (int st = 3; st < 9; ++st) {
    if (st < 4) wait_vm<N2>(); else wait_vm<N2 + 1>();
    LBAR();
    if (st == 5 && lw == 0 && lane == 0) flag_store(wsu + WS_FA / 4 + (I.pc & 3) * 64 + (I.pc >> 2) * 32 + I.rb64, tag);
    if (st == 8) {
      f = poll_issue(pfa);
      asm volatile("global_load_dwordx2 %0, %1, off sc1" : "=v"(gg) : "v"(pg) : "memory");
    }
    issueB(st);
    pf(st + 8);
    arm(st - 3);
  }
  wait_vm<N2 + 1>(); LBAR();
  poll_check(f, pfa, tag);
  issueC(0); pf(17); arm(6);
  {
    asm volatile("" : "+v"(gg));
    unsigned spins = 0;
#pragma unroll 1
    while (!__all(gg[1] == tag)) {
      __builtin_amdgcn_s_sleep(2);
      asm volatile("global_load_dwordx2 %0, %1, off sc1\n\ts_waitcnt vmcnt(0)" : "=&v"(gg) : "v"(pg) : "memory");
      if (++spins > SPIN_MAX) break;
    }
    asm volatile("ds_write_b32 %0, %1\n\ts_waitcnt lgkmcnt(0)" ::"v"((unsigned)(uintptr_t)(L + LDS_G + (lw * 64 + lane) * 4)), "v"(gg[0]) : "memory");
  }
  wait_vm<N3 + 1>(); LBAR(); issueC(1); pf(18);
  LBAR(); issueC(2); issueC(3); pf(19);
  STAMP(3);
  wait_vm<3 * N3>(); LBAR(); pf(20);
  const AS1 unsigned* pfh = wsu + WS_FH / 4 + I.rb256 * 32 + ((lane & 8) ? (lane & 3) * 8 + I.j : I.sig * 8 + (lane & 7));
#pragma unroll 1
  for (int u = 0; u < 12; ++u) {
    wait_vm<2 * N3>(); LBAR();
    if (u == 8 && lw == 0 && lane == 0) flag_store(wsu + WS_FH / 4 + I.rb256 * 32 + I.sig * 8 + I.j, tag);
    if (u == 10) f = poll_issue(pfh);
    issueC(u + 4); pf(21 + u);
  }
  wait_vm<2 * N3>(); LBAR();
  STAMP(4);
  poll_check(f, pfh, tag);
  STAMP(5);
  issueD(16); pf(33);
#pragma unroll 1
  for (int u = 13; u < 30; ++u) {
    wait_vm<2 * N3>(); LBAR();
    issueD(u + 4); pf(21 + u);
  }
  wait_vm<2 * N3>(); LBAR();
  wait_vm<N3>(); LBAR();
  wait_vm<0>(); LBAR();
  LBAR();
  STAMP(6);
  if (I.wg == 0 && lw == 1) {
    const AS1 unsigned* pa = wsu + WS_FA / 4 + lane;
#pragma unroll 1
    for (int i = 0; i < 4; ++i) poll_check(0u - (tag == 0u), pa + i * 64, tag);
    if (lane == 0) flag_store(wsu + WS_EPOCH / 4, tag);
  }
  BAR();
  BAR();
  STAMP(7);
}

template <bool Z = false>
__device__ __forceinline__ void unit(f32x4 (&acc)[4][9], const bf16x8 (&Ac)[4], bf16x8 (&An)[4], bf16x8 (&B)[7],
                                     const bf16x8 (&Xc)[2], bf16x8 (&Xn)[2], AS3 char* a, AS3 char* a2, AS3 char* b) {
  SCHED();
#pragma unroll
  for (int n = 0; n < 7; ++n) {
    MFMA_Z(acc[0][n], B[n], Ac[0]);
    SCHED();
    if (n < 4) An[n] = ld128((n < 2 ? a : a2) + (n & 1) * 2048);
    SCHED();
    MFMA_Z(acc[1][n], B[n], Ac[1]);
    SCHED();
    if (n < 2) Xn[n] = ldtr(b + (7 + n) * 32);
    SCHED();
    MFMA_Z(acc[2][n], B[n], Ac[2]);
    MFMA_Z(acc[3][n], B[n], Ac[3]);
    SCHED();
    B[n] = ldtr(b + n * 32);
    SCHED();
  }
#pragma unroll
  for (int n = 0; n < 2; ++n)
#pragma unroll
    for (int m = 0; m < 4; ++m) MFMA_Z(acc[m][7 + n], Xc[n], Ac[m]);
  SCHED();
  WAIT_LGKM0();
}
__device__ __forceinline__ void first_read(bf16x8 (&A)[4], bf16x8 (&B)[7], bf16x8 (&X)[2], AS3 char* a, AS3 char* a2, AS3 char* b) {
#pragma unroll
  for (int m = 0; m < 4; ++m) A[m] = ld128((m < 2 ? a : a2) + (m & 1) * 2048);
#pragma unroll
  for (int n = 0; n < 7; ++n) B[n] = ldtr(b + n * 32);
#pragma unroll
  for (int n = 0; n < 2; ++n) X[n] = ldtr(b + (7 + n) * 32);
  WAIT_LGKM0();
}
template <int ODD>
__device__ __forceinline__ void store_h(const f32x4 (&acc)[4][9], const float (&om)[4], rsrc_t rws, int sbase, int r, int q) {
  const int vp = ((((q & 1) << 1) | (q >> 1)) * 16 + r) * 16 + sbase + ODD * 1024;
  const int vh = ((ODD * 2 + (q >> 1)) * 16 + r) * 16 + (q & 1) * 8 + sbase + (ODD ? 0 : 4096);
#pragma unroll
  for (int m = 0; m < 4; ++m) {
    f32x4 t[9];
#pragma unroll
    for (int n = 0; n < 9; ++n)
#pragma unroll
      for (int e = 0; e < 4; ++e) t[n][e] = fmaxf(acc[m][n][e], 0.f) * om[m];
#pragma unroll
    for (int p = 0; p < 4; ++p)
      __builtin_amdgcn_raw_buffer_store_b128(swap_pair(t[2 * p + ODD], t[2 * p + 1 + ODD]), rws,
                                             vp + m * 147456 + p * 1024, 0, AUX_SC1);
    const f32x4 s = t[ODD ? 0 : 8];
    u32x2 v = {pk(s[0], s[1]), pk(s[2], s[3])};
    __builtin_amdgcn_raw_buffer_store_b64(v, rws, vh + m * 147456, 0, AUX_SC1);
  }
}

__device__ __forceinline__ void consumer(const Params& P, const Ids& I, AS3 char* L, int cw, int lane, unsigned tag) {
  const int wg = I.wg, wave = cw;
  (void)wg; (void)wave;
  const int r = lane & 15, q = lane >> 4;
  const rsrc_t rws = mk(P.ws, WS_END);
  STAMP(0);
  asm volatile("" ::"s"(tag));
  const unsigned sink = (unsigned)(uintptr_t)(L + LDS_PF) + cw * 256;
  if (PF_X && cw == 0) touch<0>(P.x, (I.wg * 72 + lane) * 128, sink);
  if (PF_X && cw == 1)
    touch<0>(lane < 8 ? P.x : (I.wg < 128 ? P.wdr : P.wdi), 0, sink, lane < 8 ? (I.wg * 72 + 64 + lane) * 128 : min((I.wg & 127) * 41 + lane - 8, 5183) * 128);
  if (PF_CODE && cw >= 2) touch<0>((const void*)I.pc0, min(((I.loc & 1) * 128 + (cw - 2) * 64 + lane) * 128, CODE_PF_BYTES), sink);
  const int aL0 = r * 128 + ((q ^ ((r >> 1) & 7)) << 4), aL1 = aL0 ^ 64;
  const int bL = (4 * q + ((lane >> 2) & 3)) * 288 + (lane & 3) * 8;
  f32x4 acc[4][9];
  {
    f32x4 lg = {0.f, 0.f, 0.f, 0.f};
#pragma unroll
    for (int n = 0; n < 9; ++n) { acc[0][n] = f32x4{0.f, 0.f, 0.f, 0.f}; asm volatile("" : "+v"(acc[0][n])); }
    asm volatile("" : "+v"(lg));
    bf16x8 xa, xb, w0[10], w1[10];
    BAR();
    STAMP(1);
    xa = ld128(L + aL0 + cw * 2048); xb = ld128(L + aL1 + cw * 2048);
#pragma unroll
    for (int n = 0; n < 9; ++n) { w0[n] = ld128(L + aL0 + 8192 + n * 2048); w1[n] = ld128(L + aL1 + 8192 + n * 2048); }
    w0[9] = ld128(L + LDS_GW + I.ra * 128 + q * 16); w1[9] = ld128(L + LDS_GW + I.ra * 128 + q * 16 + 64);
    WAIT_LGKM0();
    NOP_VALU_WRITE();
    int hs = HS;
#pragma unroll 1
    for (int st = 0; st < 9; ++st) {
      BAR();
      AS3 char* b0 = L + hs + aL0;
      AS3 char* b1 = L + hs + aL1;
      AS3 char* bg = L + LDS_GW + rot9(st + 1, I.ra) * 128 + q * 16;
      SCHED();
#pragma unroll
      for (int n = 0; n < 9; ++n) { MFMA_ACC(acc[0][n], w0[n], xa); SCHED(); w0[n] = ld128(b0 + 8192 + n * 2048); SCHED(); }
      MFMA_ACC(lg, w0[9], xa); SCHED(); w0[9] = ld128(bg); xa = ld128(b0 + cw * 2048); SCHED();
#pragma unroll
      for (int n = 0; n < 9; ++n) { MFMA_ACC(acc[0][n], w1[n], xb); SCHED(); w1[n] = ld128(b1 + 8192 + n * 2048); SCHED(); }
      MFMA_ACC(lg, w1[9], xb); SCHED(); w1[9] = ld128(bg + 64); xb = ld128(b1 + cw * 2048); SCHED();
      WAIT_LGKM0();
      hs = (hs == 3 * HS) ? 0 : hs + HS;
    }
    NOP_MFMA_READ();
    STAMP(2);
    if (q == 0 && (r >> 1) == I.pc) {
      const float g = rbf(1.f / (1.f + __expf(-rbf(lg[0]))));
      __hip_atomic_store((AS1 unsigned long long*)((AS1 char*)P.ws + WS_G) + I.rb64 * 64 + cw * 16 + r,
                         ((unsigned long long)tag << 32) | __float_as_uint(g), __ATOMIC_RELAXED, __HIP_MEMORY_SCOPE_AGENT);
    }
    const float sg = (I.pc >> 2) ? -1.f : 1.f;
    const int sl = cw * 16 + r, k = sl & 31;
    const int rho = (sl & 32) + ((k >> 2) & 1) * 16 + (k >> 3) * 4 + (k & 3);
    const int sb = WS_AB + (((I.pc >> 2) * 32 + I.rb64) * 4 + (I.pc & 3)) * BIMG;
    const int vp = sb + rho * 288 + (((q & 1) << 1) | (q >> 1)) * 16;
#pragma unroll
    for (int p = 0; p < 4; ++p)
      __builtin_amdgcn_raw_buffer_store_b128(swap_pair(acc[0][2 * p] * sg, acc[0][2 * p + 1] * sg), rws, vp + p * 64, 0, AUX_SC1);
    const f32x4 s = acc[0][8] * sg;
    u32x2 v = {pk(s[0], s[1]), pk(s[2], s[3])};
    __builtin_amdgcn_raw_buffer_store_b64(v, rws, sb + rho * 288 + 256 + q * 8, 0, AUX_SC1);
  }
  const int mh = cw >> 1, kg = cw & 1;
  bf16x8 A0[4], A1[4], B[7], X0[2], X1[2];
  {
#pragma unroll
    for (int m = 0; m < 4; ++m)
#pragma unroll
      for (int n = 0; n < 9; ++n) { acc[m][n] = f32x4{0.f, 0.f, 0.f, 0.f}; asm volatile("" : "+v"(acc[m][n])); }
    const int a0 = aL0 + cw * 8192, a1 = aL1 + cw * 8192, b0 = bL + 32768;
    BAR();
    STAMP(3);
    int sl = 2 * BS;
    first_read(A0, B, X0, L + sl + a0, L + sl + a0 + 4096, L + sl + b0);
    NOP_VALU_WRITE();
#pragma unroll 1
    for (int st = 0; st < 9; ++st) {
      if (st == 2) wait_vm<0>();
      unit(acc, A0, A1, B, X0, X1, L + sl + a1, L + sl + a1 + 4096, L + sl + b0 + 9216);
      BAR();
      sl = (sl == 0) ? 2 * BS : sl - BS;
      unit(acc, A1, A0, B, X1, X0, L + sl + a0, L + sl + a0 + 4096, L + sl + b0);
    }
    NOP_MFMA_READ();
    STAMP(4);
    float om[4];
#pragma unroll
    for (int m = 0; m < 4; ++m) {
      om[m] = rbf(1.f - *(AS3 float*)(L + LDS_G + (cw * 64 + m * 16 + r) * 4));
    }
    const int sb = WS_H + ((I.rb256 * 16 + cw * 4) * 144 + I.sig * 36 + ((I.j * 9) >> 1)) * 1024;
    if (I.j & 1) store_h<1>(acc, om, rws, sb, r, q);
    else store_h<0>(acc, om, rws, sb, r, q);
    STAMP(5);
  }
  {
    asm volatile("" : "+v"(lane));
    const int aS = (kg ? aL1 : aL0) + mh * 8192 + kg * 4096, aH = mh * 8192 + kg * 5120 + lane * 16;
    const int d2 = kg ? -4096 : 4096;
    const int bb = bL + 16384 + kg * 9216;
    BAR();
    STAMP(6);
    first_read(A0, B, X0, L + cslot(0) + aS, L + cslot(0) + aS + d2, L + cslot(0) + bb);
    BAR();
    unit<true>(acc, A0, A1, B, X0, X1, L + cslot(1) + aS, L + cslot(1) + aS + d2, L + cslot(1) + bb);
    BAR();
    unit(acc, A1, A0, B, X1, X0, L + cslot(2) + aS, L + cslot(2) + aS + d2, L + cslot(2) + bb);
#pragma unroll 1
    for (int u = 2; u < 16; u += 2) {
      if (u == 8) wait_vm<0>();
      BAR();
      const int s1 = cslot(u + 1), s2 = cslot(u + 2), an = u < 14 ? aS : aH;
      unit(acc, A0, A1, B, X0, X1, L + s1 + aS, L + s1 + aS + d2, L + s1 + bb);
      BAR();
      unit(acc, A1, A0, B, X1, X0, L + s2 + an, L + s2 + an + d2, L + s2 + bb);
    }
    NOP_MFMA_READ();
    STAMP(7);
#pragma unroll
    for (int m = 0; m < 4; ++m) {
      const float g = *(AS3 float*)(L + LDS_G + ((I.j & 1) * 128 + mh * 64 + (m ^ (2 * kg)) * 16 + (lane & 15)) * 4);
#pragma unroll
      for (int n = 0; n < 9; ++n) { acc[m][n] *= g; asm volatile("" : "+v"(acc[m][n])); }
    }
    NOP_VALU_WRITE();
#pragma unroll 1
    for (int u = 16; u < 34; u += 2) {
      BAR();
      const int s1 = cslot(u + 1), s2 = cslot(u + 2);
      unit(acc, A0, A1, B, X0, X1, L + s1 + aH, L + s1 + aH + d2, L + s1 + bb);
      BAR();
      unit(acc, A1, A0, B, X1, X0, L + s2 + aH, L + s2 + aH + d2, L + s2 + bb);
    }
    NOP_MFMA_READ();
    STAMP(8);
  }
  {
    asm volatile("" : "+v"(lane));
    AS3 char* wr = L + cw * BIMG + lane * 16;
    AS3 char* rd = L + (cw ^ 1) * BIMG + lane * 16;
#pragma unroll
    for (int m = 0; m < 2; ++m)
#pragma unroll
      for (int n = 0; n < 9; ++n) *(AS3 f32x4*)(wr + (m * 9 + n) * 1024) = acc[2 + m][n];
    WAIT_LGKM0();
    BAR();
    STAMP(10);
#pragma unroll
    for (int m = 0; m < 2; ++m)
#pragma unroll
      for (int n = 0; n < 9; ++n) acc[m][n] += *(AS3 f32x4*)(rd + (m * 9 + n) * 1024);
    if (cw == I.sig) {
      AS3 char* kp = L + 4 * BIMG + lane * 16;
#pragma unroll
      for (int m = 0; m < 2; ++m) {
#pragma unroll
        for (int n = 0; n < 8; ++n) *(AS3 f32x4*)(kp + (m * 8 + n) * 1024) = acc[m][n];
        *(AS3 f32x4*)(kp + (16 + m) * 1024) = acc[m][8];
      }
      WAIT_LGKM0();
    } else {
      const int sb = WS_XP + ((I.loc * 8 + I.rho * 4 + cw) * 3 + ((I.sig - cw - 1) & 3)) * 9216 + lane * 16;
#pragma unroll
      for (int it = 0; it < 9; ++it) {
        const f32x4 a = it < 8 ? acc[it >> 2][(it & 3) * 2] : acc[0][8];
        const f32x4 b = it < 8 ? acc[it >> 2][(it & 3) * 2 + 1] : acc[1][8];
        u32x4 v = {min(pk(a[0], a[1]), POISON - 1), min(pk(a[2], a[3]), POISON - 1), min(pk(b[0], b[1]), POISON - 1), min(pk(b[2], b[3]), POISON - 1)};
        __builtin_amdgcn_raw_buffer_store_b128(v, rws, sb + it * 1024, 0, AUX_SC1);
      }
    }
    STAMP(9);
    BAR();
  }
}

__device__ __forceinline__ void final_items(const Params& P, const Ids& I, AS3 char* L, int w, int lane) {
  const rsrc_t rws = mk(P.ws, WS_END);
  const rsrc_t ro = mk(P.out, S * D * 2);
  const int r = lane & 15, q = lane >> 4;
  const int vb = WS_XP + I.wg * 27648 + lane * 16;
  f32x4 a = *(AS3 f32x4*)(L + 4 * BIMG + w * 2048 + lane * 16);
  f32x4 b = *(AS3 f32x4*)(L + 4 * BIMG + w * 2048 + 1024 + lane * 16);
  f32x4 c = *(AS3 f32x4*)(L + 4 * BIMG + 16384 + (w & 1) * 1024 + lane * 16);
  u32x4 p[3];
  u32x2 e[3];
#pragma unroll
  for (int i = 0; i < 3; ++i) { e[i] = u32x2{0u, 0u}; p[i] = u32x4{0u, 0u, 0u, 0u}; }
  unsigned todo = 0x7;
#pragma unroll 1
  for (unsigned spins = 0; todo && spins < SPIN_MAX; ++spins) {
#pragma unroll
    for (int i = 0; i < 3; ++i)
      if (todo >> i & 1) {
        p[i] = __builtin_amdgcn_raw_buffer_load_b128(rws, vb + i * 9216 + w * 1024, 0, AUX_SC1);
        if (w < 2) e[i] = __builtin_amdgcn_raw_buffer_load_b64(rws, vb + i * 9216 + 8192 + w * 8, 0, AUX_SC1);
      }
#pragma unroll
    for (int i = 0; i < 3; ++i)
      if (todo >> i & 1) {
        const unsigned mx = max(max(max(p[i][0], p[i][1]), max(p[i][2], p[i][3])), max(e[i][0], e[i][1]));
        if (__all(mx != POISON)) todo &= ~(1u << i);
      }
    if (todo) __builtin_amdgcn_s_sleep(POLL_SLEEP);
  }
#pragma unroll
  for (int i = 0; i < 3; ++i) {
    a += f32x4{blo(p[i][0]), bhi(p[i][0]), blo(p[i][1]), bhi(p[i][1])};
    b += f32x4{blo(p[i][2]), bhi(p[i][2]), blo(p[i][3]), bhi(p[i][3])};
    c += f32x4{blo(e[i][0]), bhi(e[i][0]), blo(e[i][1]), bhi(e[i][1])};
  }
  const int row0 = I.rb128 * 128 + I.sig * 32, col0 = I.cb * 144;
  __builtin_amdgcn_raw_buffer_store_b128(swap_pair(a, b), ro, (row0 + (w >> 2) * 16 + r) * (D * 2) + (col0 + (w & 3) * 32) * 2 + (((q & 1) << 1) | (q >> 1)) * 16, 0, 0);
  if (w < 2) {
    u32x2 v = {pk(c[0], c[1]), pk(c[2], c[3])};
    __builtin_amdgcn_raw_buffer_store_b64(v, ro, (row0 + w * 16 + r) * (D * 2) + (col0 + 128) * 2 + q * 8, 0, 0);
  }
}
}

__global__ __launch_bounds__(512, 2) void gated_mix_fnet_ffn_kernel(Params P) {
  __shared__ __attribute__((aligned(1024))) char lds[LDS_BYTES];
  AS3 char* L = (AS3 char*)lds;
  const int lane = threadIdx.x & 63;
  const int wave = __builtin_amdgcn_readfirstlane(threadIdx.x >> 6);
  Ids I;
  asm volatile("s_getpc_b64 %0" : "=s"(I.pc0));
  I.wg = blockIdx.x;
  I.rho = (I.wg >> 2) & 1; I.sig = I.wg & 3; I.loc = I.wg >> 3;
  I.rb64 = 16 * I.rho + (I.loc >> 1); I.pc = 2 * I.sig + (I.loc & 1);
  I.rb256 = 4 * I.rho + (I.loc >> 3); I.j = I.loc & 7;
  I.rb128 = 2 * I.rb256 + (I.j & 1); I.cb = I.j >> 1;
  I.ra = ROT_A * ((I.rb64 & 15) % 9);
  const unsigned tag = *(const __attribute__((address_space(4))) unsigned*)((uintptr_t)P.ws + WS_EPOCH) + 1u;
  if (wave < 4) consumer(P, I, L, wave, lane, tag);
  else if (wave < 6) loader<5>(P, I, L, wave - 4, lane, tag);
  else loader<4>(P, I, L, wave - 4, lane, tag);
  {
    int tz = threadIdx.x;
    asm volatile("" : "+v"(tz));
    const int w2 = __builtin_amdgcn_readfirstlane(tz >> 6);
    __builtin_amdgcn_s_sleep(POLL_FIRST);
    final_items(P, I, L, w2, tz & 63);
  }
}

extern "C" void kernel_launch(void* const* d_in, const int* in_sizes, int n_in, void* d_out, int out_size, void* d_ws,
                              size_t ws_size, hipStream_t stream) {
  Params P;
  P.x = d_in[0]; P.wsr = d_in[1]; P.wsi = d_in[2]; P.wdr = d_in[3]; P.wdi = d_in[4];
  P.w1 = d_in[6]; P.w2 = d_in[7]; P.gw = d_in[8]; P.out = d_out; P.ws = d_ws;
  gated_mix_fnet_ffn_kernel<<<256, 512, 0, stream>>>(P);
}
```

```cpp
#include <hip/hip_runtime.h>
#include <hip/hip_bf16.h>
#include <stdint.h>

typedef __attribute__((ext_vector_type(8))) short bf16x8;
typedef __attribute__((ext_vector_type(4))) short s16x4;
typedef __attribute__((ext_vector_type(4))) float f32x4;
typedef __attribute__((ext_vector_type(2))) float f32x2;
typedef __attribute__((ext_vector_type(4))) unsigned u32x4;
typedef __attribute__((ext_vector_type(2))) unsigned u32x2;
typedef __attribute__((ext_vector_type(2))) __bf16 bf16x2_t;
#define AS3 __attribute__((address_space(3)))
#define AS1 __attribute__((address_space(1)))
typedef __amdgpu_buffer_rsrc_t rsrc_t;

namespace {
constexpr int S = 2048, D = 576, F = 4608;
constexpr int WS_EPOCH = 0;
constexpr int WS_FA = 4096;
constexpr int WS_FH = 8192;
constexpr int WS_G = 32768;
constexpr int WS_PROF = 64 << 20;
constexpr int WS_AB = 1 << 20;
constexpr int WS_XP = 6 << 20;
constexpr int WS_H = 16 << 20;
constexpr int WS_END = WS_H + 128 * 144 * 1024;
constexpr int HS = 26624;
constexpr int BS = 53248;
constexpr int LDS_PF = 3 * BS;
constexpr int LDS_GW = LDS_PF + 1024;
constexpr int LDS_G = LDS_GW + 2048;
constexpr int LDS_BYTES = LDS_G + 1024;
constexpr int BIMG = 18432;
constexpr int CODE_PF_BYTES = 29184;
constexpr unsigned SPIN_MAX = 1u << 22;
constexpr int AUX_SC1 = 16;
constexpr unsigned POISON = 0xffffffffu;

#define POLL_SLEEP 4
#define POLL_FIRST 8
#define PF_X 0
#define ROT_A 1
#define ROT_B 1
#define ROT_C 1
#define ROT_D 2
#define AUX_WS 0
#define AUX_W1 0
#define AUX_W2 0
#define AUX_WD 0
#define PF_WD 1
#define PF_CODE 1

struct Params {
  const void* x; const void* wsr; const void* wsi; const void* wdr; const void* wdi;
  const void* w1; const void* w2; const void* gw; void* out; void* ws;
};

__device__ __forceinline__ rsrc_t mk(const void* p, int bytes) {
  return __builtin_amdgcn_make_buffer_rsrc((void*)p, (short)0, bytes, 0x00020000);
}
constexpr int C0 = 124928, C1 = 69632, C2 = 0;
__device__ __forceinline__ int cnext(int s) { return s == C0 ? C1 : (s == C1 ? C2 : C0); }
__device__ __forceinline__ int hstage(int w) { return w < 2 ? 34816 + w * 13312 : (w == 2 ? 104448 : 61440); }
__device__ __forceinline__ int hpiece(int w, int i) { return hstage(w) + i * 1024 + (w == 3 && i >= 8 ? 117760 - 69632 : 0); }
__device__ __forceinline__ unsigned pk(float a, float b) {
  f32x2 v = {a, b};
  return __builtin_bit_cast(unsigned, __builtin_convertvector(v, bf16x2_t));
}
__device__ __forceinline__ float blo(unsigned u) { return __uint_as_float(u << 16); }
__device__ __forceinline__ float bhi(unsigned u) { return __uint_as_float(u & 0xffff0000u); }
__device__ __forceinline__ float rbf(float a) { return blo(pk(a, 0.f)); }
__device__ __forceinline__ bf16x8 ld128(AS3 char* p) { return *(AS3 bf16x8*)p; }
__device__ __forceinline__ bf16x8 ldtr(AS3 char* p) {
  s16x4 lo = __builtin_amdgcn_ds_read_tr16_b64_v4i16((AS3 s16x4*)p);
  s16x4 hi = __builtin_amdgcn_ds_read_tr16_b64_v4i16((AS3 s16x4*)(p + 4608));
  return __builtin_shufflevector(lo, hi, 0, 1, 2, 3, 4, 5, 6, 7);
}
#define MFMA(a, b, c) __builtin_amdgcn_mfma_f32_16x16x32_bf16(a, b, c, 0, 0, 0)
#define MFMA_ACC(c, a, b) asm volatile("v_mfma_f32_16x16x32_bf16 %0, %1, %2, %0" : "+v"(c) : "v"(a), "v"(b))
#define MFMA_NEW(c, a, b) asm volatile("v_mfma_f32_16x16x32_bf16 %0, %1, %2, 0" : "=&v"(c) : "v"(a), "v"(b))
#define MFMA_Z(c, a, b) do { if (Z) MFMA_NEW(c, a, b); else MFMA_ACC(c, a, b); } while (0)
#define NOP_MFMA_READ() do { SCHED(); asm volatile("s_nop 7\n\ts_nop 3"); SCHED(); } while (0)
#define NOP_VALU_WRITE() do { SCHED(); asm volatile("s_nop 3"); SCHED(); } while (0)
#define BAR() do { asm volatile("" ::: "memory"); __builtin_amdgcn_s_barrier(); asm volatile("" ::: "memory"); } while (0)
#define SCHED() __builtin_amdgcn_sched_barrier(0)
#define WAIT_LGKM0() __builtin_amdgcn_s_waitcnt(0xC07F)
template <int N> __device__ __forceinline__ void wait_vm() {
  asm volatile("s_waitcnt vmcnt(%0)" ::"n"(N) : "memory");
}
template <int AUX = 0>
__device__ __forceinline__ void dma(rsrc_t r, AS3 char* l, int voff, int soff) {
  __builtin_amdgcn_raw_ptr_buffer_load_lds(r, (AS3 void*)l, 16, voff, soff, 0, AUX);
}
template <int NT>
__device__ __forceinline__ void touch(const void* base, int off, unsigned lds_sink) {
  unsigned keep;
  if (NT)
    asm volatile("s_mov_b32 %0, m0\n\ts_mov_b32 m0, %3\n\ts_nop 0\n\tglobal_load_lds_dword %1, %2 nt\n\ts_mov_b32 m0, %0"
                 : "=&s"(keep) : "v"(off), "s"(base), "s"(lds_sink) : "memory");
  else
    asm volatile("s_mov_b32 %0, m0\n\ts_mov_b32 m0, %3\n\ts_nop 0\n\tglobal_load_lds_dword %1, %2\n\ts_mov_b32 m0, %0"
                 : "=&s"(keep) : "v"(off), "s"(base), "s"(lds_sink) : "memory");
}
template <int NT>
__device__ __forceinline__ void touch(const void* base, int, unsigned lds_sink, int off) {
  unsigned keep;
  asm volatile("s_mov_b32 %0, m0\n\ts_mov_b32 m0, %2\n\ts_nop 0\n\tglobal_load_lds_dword %1, off\n\ts_mov_b32 m0, %0"
               : "=&s"(keep) : "v"((const char*)base + off), "s"(lds_sink) : "memory");
}
__device__ __forceinline__ u32x4 swap_pair(const f32x4& a, const f32x4& b) {
  unsigned x0 = pk(a[0], a[1]), x1 = pk(a[2], a[3]), y0 = pk(b[0], b[1]), y1 = pk(b[2], b[3]);
  auto s0 = __builtin_amdgcn_permlane16_swap(x0, y0, false, false);
  auto s1 = __builtin_amdgcn_permlane16_swap(x1, y1, false, false);
  u32x4 v = {s0[0], s1[0], s0[1], s1[1]};
  return v;
}
__device__ __forceinline__ unsigned poll_issue(const AS1 unsigned* p) {
  unsigned f;
  asm volatile("global_load_dword %0, %1, off sc1" : "=v"(f) : "v"(p) : "memory");
  return f;
}
__device__ __forceinline__ void poll_check(unsigned f, const AS1 unsigned* p, unsigned tag) {
  asm volatile("" : "+v"(f));
  unsigned spins = 0;
#pragma unroll 1
  while (!__all(f == tag)) {
    __builtin_amdgcn_s_sleep(2);
    asm volatile("global_load_dword %0, %1, off sc1\n\ts_waitcnt vmcnt(0)" : "=&v"(f) : "v"(p) : "memory");
    if (++spins > SPIN_MAX) break;
  }
}
__device__ __forceinline__ void flag_store(AS1 unsigned* p, unsigned v) {
  __hip_atomic_store(p, v, __ATOMIC_RELAXED, __HIP_MEMORY_SCOPE_AGENT);
}

#define RSTAMP(i) do {} while (0)
#define STAMP(i) do {} while (0)

__device__ __forceinline__ int rot9(int st, int k) { const int t = st + k; return t >= 9 ? t - 9 : t; }
__device__ __forceinline__ int rot4(int st, int k, int lim) { return st < lim ? (st & ~3) | ((st + k) & 3) : st; }
struct Ids { uint64_t pc0; int wg, rho, sig, loc, rb64, pc, rb256, j, rb128, cb, ra; };

#define LBAR() BAR()
template <int NB>
__device__ __forceinline__ void loader(const Params& P, const Ids& I, AS3 char* L, int lw, int lane, unsigned tag) {
  const int wg = I.wg, wave = lw + 4;
  (void)wg; (void)wave;
  constexpr int N1 = 2 + NB, N2 = 8 + NB, N3 = 4 + NB;
  const rsrc_t rx = mk(P.x, S * D * 2);
  const rsrc_t rwd = mk((I.pc >> 2) ? P.wdi : P.wdr, D * D * 2);
  const rsrc_t rw1 = mk(P.w1, D * F * 2);
  const rsrc_t rw2 = mk(P.w2, F * D * 2);
  const rsrc_t rs = mk((I.sig >> 1) ? P.wsi : P.wsr, S * S * 2);
  const rsrc_t rws = mk(P.ws, WS_END);
  const int sw = ((lane & 7) ^ (((lw & 1) << 2) + (lane >> 4))) << 4;
  const int vx = (lw * 8 + (lane >> 3)) * (D * 2) + sw;
  const int vs = (lw * 8 + (lane >> 3)) * (S * 2) + sw;
  const int vl = lane * 16;
  int v1[NB], v2[NB];
#pragma unroll
  for (int i = 0; i < NB; ++i) {
    int c = (lw + 4 * i) * 64 + lane, r = c / 18, col = c - r * 18;
    int k = (r & 32) + ((r >> 2) & 3) * 8 + ((r >> 4) & 1) * 4 + (r & 3);
    v1[i] = k * (F * 2) + col * 16;
    v2[i] = k * (D * 2) + col * 16;
  }
  auto issueAx = [&](int st) {
    AS3 char* b = L + (st & 3) * HS + lw * 1024;
#pragma unroll
    for (int i = 0; i < 2; ++i) dma(rx, b + i * 4096, vx, (I.rb64 * 64 + i * 32) * (D * 2) + rot9(st, I.ra) * 128);
  };
  auto issueAw = [&](int st) {
    AS3 char* b = L + (st & 3) * HS + lw * 1024;
#pragma unroll
    for (int i = 0; i < NB; ++i) dma<AUX_WD>(rwd, b + 8192 + i * 4096, vx, ((I.pc & 3) * 144 + i * 32) * (D * 2) + rot9(st, I.ra) * 128);
  };
  auto issueA = [&](int st) { issueAx(st); issueAw(st); };
  auto issueBw = [&](int st) {
    AS3 char* b = L + (2 - st % 3) * BS + lw * 1024;
#pragma unroll
    for (int i = 0; i < NB; ++i) dma<AUX_W1>(rw1, b + 32768 + i * 4096, v1[i], rot4(st, ROT_B * I.rb256, 8) * 64 * (F * 2) + (I.sig * 8 + I.j) * 288);
  };
  auto issueBx = [&](int st) {
    AS3 char* b = L + (2 - st % 3) * BS + lw * 1024;
#pragma unroll
    for (int i = 0; i < 8; ++i) dma(rx, b + i * 4096, vx, (I.rb256 * 256 + i * 32) * (D * 2) + rot4(st, ROT_B * I.rb256, 8) * 128);
  };
  auto issueB = [&](int st) { issueBw(st); issueBx(st); };
  auto issueC = [&](int st, int sl) {
    AS3 char* b = L + sl + lw * 1024;
#pragma unroll
    for (int i = 0; i < 4; ++i) {
      const int so = (I.rb128 * 128 + i * 32) * (S * 2) + (I.sig & 1) * 2048 + rot4(st, ROT_C * I.cb, 16) * 128;
      dma<AUX_WS>(rs, b + i * 4096, vs, so);
    }
#pragma unroll
    for (int i = 0; i < NB; ++i) dma<AUX_SC1>(rws, b + 16384 + i * 4096, vl, WS_AB + ((I.sig * 16 + rot4(st, ROT_C * I.cb, 16)) * 4 + I.cb) * BIMG + (lw + 4 * i) * 1024);
  };
  auto issueD = [&](int st, int sl) {
    AS3 char* b = L + sl + lw * 1024;
    const int t = ROT_D == 2 ? (st < 32 ? ((st - 16) & 8) | ((st + I.rb128) & 7) : st - 16) : rot4(st - 16, ROT_D * I.rb128, 16);
#pragma unroll
    for (int i = 0; i < NB; ++i) dma<AUX_W2>(rw2, b + 16384 + i * 4096, v2[i], (I.sig * 1152 + t * 64) * (D * 2) + I.cb * 288);
#pragma unroll
    for (int i = 0; i < 4; ++i)
      dma<AUX_SC1>(rws, b + i * 4096, vl, WS_H + ((I.rb128 * 8 + 2 * i + (lw >> 1)) * 144 + I.sig * 36 + 2 * t + (lw & 1)) * 1024);
  };
  AS1 unsigned* wsu = (AS1 unsigned*)P.ws;

  auto arm = [&](int i) {
    const u32x4 pz = {POISON, POISON, POISON, POISON};
    __builtin_amdgcn_raw_buffer_store_b128(pz, rws, WS_XP + I.wg * 27648 + min(lw * 7 + i, 26) * 1024 + vl, 0, AUX_SC1);
  };
  STAMP(0); RSTAMP(13);
  if (lw >= 2) dma(mk(P.gw, D * 2), L + LDS_GW + (lw - 2) * 1024, vl + (lw - 2) * 1024, 0);
  int bi = 0; (void)bi;
  issueA(0); issueA(1); issueA(2);
  asm volatile("" ::"s"(tag));
  wait_vm<2 * N1>(); LBAR(); issueA(3);
  STAMP(1);
#pragma unroll 1
  for (int k = 0; k < 5; ++k) { wait_vm<2 * N1>(); LBAR(); issueA(k + 4); }
  wait_vm<2 * N1>(); LBAR(); issueBw(0);
  wait_vm<N1 + NB>(); LBAR(); issueBx(0);
  wait_vm<N2>(); LBAR(); issueBw(1);
  LBAR(); issueBx(1);
  STAMP(2);
  wait_vm<N2>(); LBAR(); issueB(2);
  const AS1 unsigned* pfa = wsu + WS_FA / 4 + I.cb * 64 + I.sig * 16 + (lane & 15);
  const AS1 unsigned long long* pg = (const AS1 unsigned long long*)(wsu + WS_G / 4) + I.rb256 * 256 + lw * 64 + lane;
  unsigned f = 0;
  u32x2 gg = {0u, 0u};
#pragma unroll 1
  for (int st = 3; st < 9; ++st) {
    wait_vm<N2>(); LBAR();
    if (st == 5 && lw == 0 && lane == 0) flag_store(wsu + WS_FA / 4 + (I.pc & 3) * 64 + (I.pc >> 2) * 32 + I.rb64, tag);
    if (st == 8) {
      f = poll_issue(pfa);
      asm volatile("global_load_dwordx2 %0, %1, off sc1" : "=v"(gg) : "v"(pg) : "memory");
    }
    arm(st - 3);
    issueB(st);
  }
  wait_vm<N2>(); LBAR();
  poll_check(f, pfa, tag);
  arm(6); issueC(0, C0);
  {
    asm volatile("" : "+v"(gg));
    unsigned spins = 0;
#pragma unroll 1
    while (!__all(gg[1] == tag)) {
      __builtin_amdgcn_s_sleep(2);
      asm volatile("global_load_dwordx2 %0, %1, off sc1\n\ts_waitcnt vmcnt(0)" : "=&v"(gg) : "v"(pg) : "memory");
      if (++spins > SPIN_MAX) break;
    }
    asm volatile("ds_write_b32 %0, %1\n\ts_waitcnt lgkmcnt(0)" ::"v"((unsigned)(uintptr_t)(L + LDS_G + (lw * 64 + lane) * 4)), "v"(gg[0]) : "memory");
  }
  wait_vm<N3>(); LBAR(); issueC(1, C1);
  LBAR(); issueC(2, C2);
  STAMP(3);
  const int hq = lane >> 4;
  const int hv = WS_H + ((I.rb256 * 16 + lw * 4) * 144 + I.sig * 36 + ((I.j * 9 + 1) >> 1)) * 1024 + ((((hq & 1) << 1) | (hq >> 1)) * 16 + (lane & 15)) * 16;
  auto drain = [&](int i) {
    u32x4 v0, v1;
    const int i1 = min(i + 1, 12);
    asm volatile("ds_read_b128 %0, %2\n\tds_read_b128 %1, %3\n\ts_waitcnt lgkmcnt(0)" : "=&v"(v0), "=&v"(v1)
                 : "v"((unsigned)(uintptr_t)L + hpiece(lw, i) + vl), "v"((unsigned)(uintptr_t)L + hpiece(lw, i1) + vl) : "memory");
    __builtin_amdgcn_raw_buffer_store_b128(v0, rws, hv + ((i + 3) >> 2) * 147456 + ((i + 3) & 3) * 1024, 0, AUX_SC1);
    __builtin_amdgcn_raw_buffer_store_b128(v1, rws, hv + ((i1 + 3) >> 2) * 147456 + ((i1 + 3) & 3) * 1024, 0, AUX_SC1);
  };
  wait_vm<2 * N3>(); LBAR(); drain(0);
  const AS1 unsigned* pfh = wsu + WS_FH / 4 + I.rb256 * 32 + ((lane & 8) ? (lane & 3) * 8 + I.j : I.sig * 8 + (lane & 7));
  int cs = C0;
#pragma unroll 1
  for (int u = 0; u < 13; ++u) {
    if (u == 8) wait_vm<0>(); else wait_vm<N3>();
    LBAR();
    if (u == 8 && lw == 0 && lane == 0) flag_store(wsu + WS_FH / 4 + I.rb256 * 32 + I.sig * 8 + I.j, tag);
    if (u == 12) f = poll_issue(pfh);
    if (u < 6) drain(2 * u + 2);
    issueC(u + 3, cs);
    cs = cnext(cs);
  }
  wait_vm<N3>(); LBAR();
  STAMP(4);
  poll_check(f, pfh, tag);
  STAMP(5);
  issueD(16, cs);
  cs = cnext(cs);
#pragma unroll 1
  for (int u = 14; u < 31; ++u) {
    wait_vm<N3>(); LBAR();
    issueD(u + 3, cs);
    cs = cnext(cs);
  }
  wait_vm<N3>(); LBAR();
  wait_vm<0>(); LBAR();
  LBAR();
  STAMP(6);
  if (I.wg == 0 && lw == 1) {
    const AS1 unsigned* pa = wsu + WS_FA / 4 + lane;
#pragma unroll 1
    for (int i = 0; i < 4; ++i) poll_check(0u - (tag == 0u), pa + i * 64, tag);
    if (lane == 0) flag_store(wsu + WS_EPOCH / 4, tag);
  }
  BAR();
  BAR();
  STAMP(7);
}

template <bool Z = false>
__device__ __forceinline__ void unit(f32x4 (&acc)[4][9], const bf16x8 (&Ac)[4], bf16x8 (&An)[4], bf16x8 (&B)[7],
                                     const bf16x8 (&Xc)[2], bf16x8 (&Xn)[2], AS3 char* a, AS3 char* a2, AS3 char* b) {
  SCHED();
#pragma unroll
  for (int n = 0; n < 7; ++n) {
    MFMA_Z(acc[0][n], B[n], Ac[0]);
    SCHED();
    if (n < 4) An[n] = ld128((n < 2 ? a : a2) + (n & 1) * 2048);
    SCHED();
    MFMA_Z(acc[1][n], B[n], Ac[1]);
    SCHED();
    if (n < 2) Xn[n] = ldtr(b + (7 + n) * 32);
    SCHED();
    MFMA_Z(acc[2][n], B[n], Ac[2]);
    MFMA_Z(acc[3][n], B[n], Ac[3]);
    SCHED();
    B[n] = ldtr(b + n * 32);
    SCHED();
  }
#pragma unroll
  for (int n = 0; n < 2; ++n)
#pragma unroll
    for (int m = 0; m < 4; ++m) MFMA_Z(acc[m][7 + n], Xc[n], Ac[m]);
  SCHED();
  WAIT_LGKM0();
}
__device__ __forceinline__ void first_read(bf16x8 (&A)[4], bf16x8 (&B)[7], bf16x8 (&X)[2], AS3 char* a, AS3 char* a2, AS3 char* b) {
#pragma unroll
  for (int m = 0; m < 4; ++m) A[m] = ld128((m < 2 ? a : a2) + (m & 1) * 2048);
#pragma unroll
  for (int n = 0; n < 7; ++n) B[n] = ldtr(b + n * 32);
#pragma unroll
  for (int n = 0; n < 2; ++n) X[n] = ldtr(b + (7 + n) * 32);
  WAIT_LGKM0();
}
template <int ODD>
__device__ __forceinline__ void store_h(const f32x4 (&acc)[4][9], const float (&om)[4], rsrc_t rws, int sbase, int r, int q,
                                        AS3 char* st0, AS3 char* st1) {
  const int vp = ((((q & 1) << 1) | (q >> 1)) * 16 + r) * 16 + sbase + ODD * 1024;
  const int vh = ((ODD * 2 + (q >> 1)) * 16 + r) * 16 + (q & 1) * 8 + sbase + (ODD ? 0 : 4096);
#pragma unroll
  for (int m = 0; m < 4; ++m) {
    f32x4 t[9];
#pragma unroll
    for (int n = 0; n < 9; ++n)
#pragma unroll
      for (int e = 0; e < 4; ++e) t[n][e] = fmaxf(acc[m][n][e], 0.f) * om[m];
#pragma unroll
    for (int p = 0; p < 4; ++p) {
      const u32x4 v = swap_pair(t[2 * p + ODD], t[2 * p + 1 + ODD]);
      const int i = m * 4 + p - 3;
      if (i < 0) __builtin_amdgcn_raw_buffer_store_b128(v, rws, vp + m * 147456 + p * 1024, 0, AUX_SC1);
      else *(AS3 u32x4*)((i < 8 ? st0 : st1) + i * 1024) = v;
    }
    const f32x4 s = t[ODD ? 0 : 8];
    u32x2 v = {pk(s[0], s[1]), pk(s[2], s[3])};
    __builtin_amdgcn_raw_buffer_store_b64(v, rws, vh + m * 147456, 0, AUX_SC1);
  }
}

__device__ __forceinline__ void consumer(const Params& P, const Ids& I, AS3 char* L, int cw, int lane, unsigned tag) {
  const int wg = I.wg, wave = cw;
  (void)wg; (void)wave;
  const int r = lane & 15, q = lane >> 4;
  const rsrc_t rws = mk(P.ws, WS_END);
  STAMP(0);
  asm volatile("" ::"s"(tag));
  const unsigned sink = (unsigned)(uintptr_t)(L + LDS_PF) + cw * 256;
  if (PF_X && cw == 0) touch<0>(P.x, (I.wg * 72 + lane) * 128, sink);
  if (PF_X && cw == 1)
    touch<0>(lane < 8 ? P.x : (I.wg < 128 ? P.wdr : P.wdi), 0, sink, lane < 8 ? (I.wg * 72 + 64 + lane) * 128 : min((I.wg & 127) * 41 + lane - 8, 5183) * 128);
  const int aL0 = r * 128 + ((q ^ ((r >> 1) & 7)) << 4), aL1 = aL0 ^ 64;
  const int bL = (4 * q + ((lane >> 2) & 3)) * 288 + (lane & 3) * 8;
  f32x4 acc[4][9];
  {
    f32x4 lg = {0.f, 0.f, 0.f, 0.f};
#pragma unroll
    for (int n = 0; n < 9; ++n) { acc[0][n] = f32x4{0.f, 0.f, 0.f, 0.f}; asm volatile("" : "+v"(acc[0][n])); }
    asm volatile("" : "+v"(lg));
#pragma unroll
    for (int m = 1; m < 4; ++m)
#pragma unroll
      for (int n = 0; n < 9; ++n) { acc[m][n] = f32x4{0.f, 0.f, 0.f, 0.f}; asm volatile("" : "+v"(acc[m][n])); }
    bf16x8 xa, xb, w0[10], w1[10];
    BAR();
    STAMP(1);
    xa = ld128(L + aL0 + cw * 2048); xb = ld128(L + aL1 + cw * 2048);
#pragma unroll
    for (int n = 0; n < 9; ++n) { w0[n] = ld128(L + aL0 + 8192 + n * 2048); w1[n] = ld128(L + aL1 + 8192 + n * 2048); }
    w0[9] = ld128(L + LDS_GW + I.ra * 128 + q * 16); w1[9] = ld128(L + LDS_GW + I.ra * 128 + q * 16 + 64);
    WAIT_LGKM0();
    NOP_VALU_WRITE();
    int hs = HS;
#pragma unroll 1
    for (int st = 0; st < 9; ++st) {
      BAR();
      AS3 char* b0 = L + hs + aL0;
      AS3 char* b1 = L + hs + aL1;
      AS3 char* bg = L + LDS_GW + rot9(st + 1, I.ra) * 128 + q * 16;
      SCHED();
#pragma unroll
      for (int n = 0; n < 9; ++n) { MFMA_ACC(acc[0][n], w0[n], xa); SCHED(); w0[n] = ld128(b0 + 8192 + n * 2048); SCHED(); }
      MFMA_ACC(lg, w0[9], xa); SCHED(); w0[9] = ld128(bg); xa = ld128(b0 + cw * 2048); SCHED();
#pragma unroll
      for (int n = 0; n < 9; ++n) { MFMA_ACC(acc[0][n], w1[n], xb); SCHED(); w1[n] = ld128(b1 + 8192 + n * 2048); SCHED(); }
      MFMA_ACC(lg, w1[9], xb); SCHED(); w1[9] = ld128(bg + 64); xb = ld128(b1 + cw * 2048); SCHED();
      WAIT_LGKM0();
      hs = (hs == 3 * HS) ? 0 : hs + HS;
    }
    NOP_MFMA_READ();
    STAMP(2);
    if (q == 0 && (r >> 1) == I.pc) {
      const float g = rbf(1.f / (1.f + __expf(-rbf(lg[0]))));
      __hip_atomic_store((AS1 unsigned long long*)((AS1 char*)P.ws + WS_G) + I.rb64 * 64 + cw * 16 + r,
                         ((unsigned long long)tag << 32) | __float_as_uint(g), __ATOMIC_RELAXED, __HIP_MEMORY_SCOPE_AGENT);
    }
    const float sg = (I.pc >> 2) ? -1.f : 1.f;
    const int sl = cw * 16 + r, k = sl & 31;
    const int rho = (sl & 32) + ((k >> 2) & 1) * 16 + (k >> 3) * 4 + (k & 3);
    const int sb = WS_AB + (((I.pc >> 2) * 32 + I.rb64) * 4 + (I.pc & 3)) * BIMG;
    const int vp = sb + rho * 288 + (((q & 1) << 1) | (q >> 1)) * 16;
#pragma unroll
    for (int p = 0; p < 4; ++p)
      __builtin_amdgcn_raw_buffer_store_b128(swap_pair(acc[0][2 * p] * sg, acc[0][2 * p + 1] * sg), rws, vp + p * 64, 0, AUX_SC1);
    const f32x4 s = acc[0][8] * sg;
    u32x2 v = {pk(s[0], s[1]), pk(s[2], s[3])};
    __builtin_amdgcn_raw_buffer_store_b64(v, rws, sb + rho * 288 + 256 + q * 8, 0, AUX_SC1);
  }
  const int mh = cw >> 1, kg = cw & 1;
  bf16x8 A0[4], A1[4], B[7], X0[2], X1[2];
  {
#pragma unroll
    for (int n = 0; n < 9; ++n) { acc[0][n] = f32x4{0.f, 0.f, 0.f, 0.f}; asm volatile("" : "+v"(acc[0][n])); }
    const int a0 = aL0 + cw * 8192, a1 = aL1 + cw * 8192, b0 = bL + 32768;
    BAR();
    STAMP(3);
    int sl = 2 * BS;
    first_read(A0, B, X0, L + sl + a0, L + sl + a0 + 4096, L + sl + b0);
    NOP_VALU_WRITE();
#pragma unroll 1
    for (int st = 0; st < 9; ++st) {
      if (st == 2) wait_vm<0>();
      unit(acc, A0, A1, B, X0, X1, L + sl + a1, L + sl + a1 + 4096, L + sl + b0 + 9216);
      BAR();
      sl = (sl == 0) ? 2 * BS : sl - BS;
      unit(acc, A1, A0, B, X1, X0, L + sl + a0, L + sl + a0 + 4096, L + sl + b0);
    }
    NOP_MFMA_READ();
    STAMP(4);
    float om[4];
#pragma unroll
    for (int m = 0; m < 4; ++m) {
      om[m] = rbf(1.f - *(AS3 float*)(L + LDS_G + (cw * 64 + m * 16 + r) * 4));
    }
    const int sb = WS_H + ((I.rb256 * 16 + cw * 4) * 144 + I.sig * 36 + ((I.j * 9) >> 1)) * 1024;
    AS3 char* st0 = L + hpiece(cw, 0) + lane * 16;
    AS3 char* st1 = L + hpiece(cw, 8) - 8192 + lane * 16;
    if (I.j & 1) store_h<1>(acc, om, rws, sb, r, q, st0, st1);
    else store_h<0>(acc, om, rws, sb, r, q, st0, st1);
    WAIT_LGKM0();
    STAMP(5);
  }
  {
    asm volatile("" : "+v"(lane));
    const int aS = (kg ? aL1 : aL0) + mh * 8192 + kg * 4096, aH = mh * 8192 + kg * 5120 + lane * 16;
    const int d2 = kg ? -4096 : 4096;
    const int bb = bL + 16384 + kg * 9216;
    BAR();
    STAMP(6);
    first_read(A0, B, X0, L + C0 + aS, L + C0 + aS + d2, L + C0 + bb);
    BAR();
    unit<true>(acc, A0, A1, B, X0, X1, L + C1 + aS, L + C1 + aS + d2, L + C1 + bb);
    BAR();
    unit(acc, A1, A0, B, X1, X0, L + C2 + aS, L + C2 + aS + d2, L + C2 + bb);
    int s2 = C2;
#pragma unroll 1
    for (int u = 2; u < 16; u += 2) {
      if (u == 8) wait_vm<0>();
      BAR();
      const int s1 = cnext(s2), an = u < 14 ? aS : aH;
      s2 = cnext(s1);
      unit(acc, A0, A1, B, X0, X1, L + s1 + aS, L + s1 + aS + d2, L + s1 + bb);
      BAR();
      unit(acc, A1, A0, B, X1, X0, L + s2 + an, L + s2 + an + d2, L + s2 + bb);
    }
    NOP_MFMA_READ();
    STAMP(7);
#pragma unroll
    for (int m = 0; m < 4; ++m) {
      const float g = *(AS3 float*)(L + LDS_G + ((I.j & 1) * 128 + mh * 64 + (m ^ (2 * kg)) * 16 + (lane & 15)) * 4);
#pragma unroll
      for (int n = 0; n < 9; ++n) { acc[m][n] *= g; asm volatile("" : "+v"(acc[m][n])); }
    }
    NOP_VALU_WRITE();
#pragma unroll 1
    for (int u = 16; u < 34; u += 2) {
      BAR();
      const int s1 = cnext(s2);
      s2 = cnext(s1);
      unit(acc, A0, A1, B, X0, X1, L + s1 + aH, L + s1 + aH + d2, L + s1 + bb);
      BAR();
      unit(acc, A1, A0, B, X1, X0, L + s2 + aH, L + s2 + aH + d2, L + s2 + bb);
    }
    NOP_MFMA_READ();
    STAMP(8);
  }
  {
    asm volatile("" : "+v"(lane));
    AS3 char* wr = L + cw * BIMG + lane * 16;
    AS3 char* rd = L + (cw ^ 1) * BIMG + lane * 16;
#pragma unroll
    for (int m = 0; m < 2; ++m)
#pragma unroll
      for (int n = 0; n < 9; ++n) *(AS3 f32x4*)(wr + (m * 9 + n) * 1024) = acc[2 + m][n];
    WAIT_LGKM0();
    BAR();
    STAMP(10);
#pragma unroll
    for (int m = 0; m < 2; ++m)
#pragma unroll
      for (int n = 0; n < 9; ++n) acc[m][n] += *(AS3 f32x4*)(rd + (m * 9 + n) * 1024);
    if (cw == I.sig) {
      AS3 char* kp = L + 4 * BIMG + lane * 16;
#pragma unroll
      for (int m = 0; m < 2; ++m) {
#pragma unroll
        for (int n = 0; n < 8; ++n) *(AS3 f32x4*)(kp + (m * 8 + n) * 1024) = acc[m][n];
        *(AS3 f32x4*)(kp + (16 + m) * 1024) = acc[m][8];
      }
      WAIT_LGKM0();
    } else {
      const int sb = WS_XP + ((I.loc * 8 + I.rho * 4 + cw) * 3 + ((I.sig - cw - 1) & 3)) * 9216 + lane * 16;
#pragma unroll
      for (int it = 0; it < 9; ++it) {
        const f32x4 a = it < 8 ? acc[it >> 2][(it & 3) * 2] : acc[0][8];
        const f32x4 b = it < 8 ? acc[it >> 2][(it & 3) * 2 + 1] : acc[1][8];
        u32x4 v = {min(pk(a[0], a[1]), POISON - 1), min(pk(a[2], a[3]), POISON - 1), min(pk(b[0], b[1]), POISON - 1), min(pk(b[2], b[3]), POISON - 1)};
        __builtin_amdgcn_raw_buffer_store_b128(v, rws, sb + it * 1024, 0, AUX_SC1);
      }
    }
    STAMP(9);
    BAR();
  }
}

__device__ __forceinline__ void final_items(const Params& P, const Ids& I, AS3 char* L, int w, int lane) {
  const rsrc_t rws = mk(P.ws, WS_END);
  const rsrc_t ro = mk(P.out, S * D * 2);
  const int r = lane & 15, q = lane >> 4;
  const int vb = WS_XP + I.wg * 27648 + lane * 16;
  f32x4 a = *(AS3 f32x4*)(L + 4 * BIMG + w * 2048 + lane * 16);
  f32x4 b = *(AS3 f32x4*)(L + 4 * BIMG + w * 2048 + 1024 + lane * 16);
  f32x4 c = *(AS3 f32x4*)(L + 4 * BIMG + 16384 + (w & 1) * 1024 + lane * 16);
  u32x4 p[3];
  u32x2 e[3];
#pragma unroll
  for (int i = 0; i < 3; ++i) { e[i] = u32x2{0u, 0u}; p[i] = u32x4{0u, 0u, 0u, 0u}; }
  unsigned todo = 0x7;
#pragma unroll 1
  for (unsigned spins = 0; todo && spins < SPIN_MAX; ++spins) {
#pragma unroll
    for (int i = 0; i < 3; ++i)
      if (todo >> i & 1) {
        p[i] = __builtin_amdgcn_raw_buffer_load_b128(rws, vb + i * 9216 + w * 1024, 0, AUX_SC1);
        if (w < 2) e[i] = __builtin_amdgcn_raw_buffer_load_b64(rws, vb + i * 9216 + 8192 + w * 8, 0, AUX_SC1);
      }
#pragma unroll
    for (int i = 0; i < 3; ++i)
      if (todo >> i & 1) {
        const unsigned mx = max(max(max(p[i][0], p[i][1]), max(p[i][2], p[i][3])), max(e[i][0], e[i][1]));
        if (__all(mx != POISON)) todo &= ~(1u << i);
      }
    if (todo) __builtin_amdgcn_s_sleep(POLL_SLEEP);
  }
#pragma unroll
  for (int i = 0; i < 3; ++i) {
    a += f32x4{blo(p[i][0]), bhi(p[i][0]), blo(p[i][1]), bhi(p[i][1])};
    b += f32x4{blo(p[i][2]), bhi(p[i][2]), blo(p[i][3]), bhi(p[i][3])};
    c += f32x4{blo(e[i][0]), bhi(e[i][0]), blo(e[i][1]), bhi(e[i][1])};
  }
  const int row0 = I.rb128 * 128 + I.sig * 32, col0 = I.cb * 144;
  __builtin_amdgcn_raw_buffer_store_b128(swap_pair(a, b), ro, (row0 + (w >> 2) * 16 + r) * (D * 2) + (col0 + (w & 3) * 32) * 2 + (((q & 1) << 1) | (q >> 1)) * 16, 0, 0);
  if (w < 2) {
    u32x2 v = {pk(c[0], c[1]), pk(c[2], c[3])};
    __builtin_amdgcn_raw_buffer_store_b64(v, ro, (row0 + w * 16 + r) * (D * 2) + (col0 + 128) * 2 + q * 8, 0, 0);
  }
}
}

__global__ __launch_bounds__(512, 2) void gated_mix_fnet_ffn_kernel(Params P) {
  __shared__ __attribute__((aligned(1024))) char lds[LDS_BYTES];
  AS3 char* L = (AS3 char*)lds;
  const int lane = threadIdx.x & 63;
  const int wave = __builtin_amdgcn_readfirstlane(threadIdx.x >> 6);
  Ids I;
  asm volatile("s_getpc_b64 %0" : "=s"(I.pc0));
  I.wg = blockIdx.x;
  if (PF_CODE && wave == 0)
    touch<0>((const void*)I.pc0, min(((I.wg >> 3) * 8 + (lane & 7)) * 128, CODE_PF_BYTES), (unsigned)(uintptr_t)(L + LDS_PF));
  I.rho = (I.wg >> 2) & 1; I.sig = I.wg & 3; I.loc = I.wg >> 3;
  I.rb64 = 16 * I.rho + (I.loc >> 1); I.pc = 2 * I.sig + (I.loc & 1);
  I.rb256 = 4 * I.rho + (I.loc >> 3); I.j = I.loc & 7;
  I.rb128 = 2 * I.rb256 + (I.j & 1); I.cb = I.j >> 1;
  I.ra = ROT_A * ((I.rb64 & 15) % 9);
  const unsigned tag = *(const __attribute__((address_space(4))) unsigned*)((uintptr_t)P.ws + WS_EPOCH) + 1u;
  if (wave < 4) consumer(P, I, L, wave, lane, tag);
  else if (wave < 6) loader<5>(P, I, L, wave - 4, lane, tag);
  else loader<4>(P, I, L, wave - 4, lane, tag);
  {
    int tz = threadIdx.x;
    asm volatile("" : "+v"(tz));
    const int w2 = __builtin_amdgcn_readfirstlane(tz >> 6);
    __builtin_amdgcn_s_sleep(POLL_FIRST);
    final_items(P, I, L, w2, tz & 63);
  }
}

extern "C" void kernel_launch(void* const* d_in, const int* in_sizes, int n_in, void* d_out, int out_size, void* d_ws,
                              size_t ws_size, hipStream_t stream) {
  Params P;
  P.x = d_in[0]; P.wsr = d_in[1]; P.wsi = d_in[2]; P.wdr = d_in[3]; P.wdi = d_in[4];
  P.w1 = d_in[6]; P.w2 = d_in[7]; P.gw = d_in[8]; P.out = d_out; P.ws = d_ws;
  gated_mix_fnet_ffn_kernel<<<256, 512, 0, stream>>>(P);
}
```

```cpp
#include <hip/hip_runtime.h>
#include <hip/hip_bf16.h>
#include <stdint.h>

typedef __attribute__((ext_vector_type(8))) short bf16x8;
typedef __attribute__((ext_vector_type(4))) short s16x4;
typedef __attribute__((ext_vector_type(4))) float f32x4;
typedef __attribute__((ext_vector_type(2))) float f32x2;
typedef __attribute__((ext_vector_type(4))) unsigned u32x4;
typedef __attribute__((ext_vector_type(2))) unsigned u32x2;
typedef __attribute__((ext_vector_type(2))) __bf16 bf16x2_t;
#define AS3 __attribute__((address_space(3)))
#define AS1 __attribute__((address_space(1)))
typedef __amdgpu_buffer_rsrc_t rsrc_t;

namespace {
constexpr int S = 2048, D = 576, F = 4608;
constexpr int WS_EPOCH = 0;
constexpr int WS_FA = 4096;
constexpr int WS_FH = 8192;
constexpr int WS_G = 32768;
constexpr int WS_PROF = 64 << 20;
constexpr int WS_AB = 1 << 20;
constexpr int WS_XP = 6 << 20;
constexpr int WS_H = 16 << 20;
constexpr int WS_END = WS_H + 128 * 144 * 1024;
constexpr int HS = 26624;
constexpr int BS = 53248;
constexpr int LDS_PF = 3 * BS;
constexpr int LDS_GW = LDS_PF + 1024;
constexpr int LDS_G = LDS_GW + 2048;
constexpr int LDS_BYTES = LDS_G + 1024;
constexpr int BIMG = 18432;
constexpr int CODE_PF_BYTES = 28672;
constexpr unsigned SPIN_MAX = 1u << 22;
constexpr int AUX_SC1 = 16;
constexpr unsigned POISON = 0xffffffffu;

#define POLL_SLEEP 4
#define POLL_FIRST 8
#define PF_X 1
#define ROT_A 1
#define ROT_B 1
#define ROT_C 1
#define ROT_D 2
#define AUX_WS 0
#define AUX_W1 0
#define AUX_W2 0
#define AUX_WD 0
#define PF_WD 1
#define PF_W1 0
#define PF_WS 1
#define PF_W2 0
#define NT_W1 0
#define NT_WS 0
#define NT_W2 0
#define W1_0 3
#define W1_S 1
#define WS_0 12
#define WS_S 1
#define W2_0 23
#define W2_S 2
#define PF_WSN 8
#define PF_CL 8

struct Params {
  const void* x; const void* wsr; const void* wsi; const void* wdr; const void* wdi;
  const void* w1; const void* w2; const void* gw; void* out; void* ws;
};

__device__ __forceinline__ rsrc_t mk(const void* p, int bytes) {
  return __builtin_amdgcn_make_buffer_rsrc((void*)p, (short)0, bytes, 0x00020000);
}
__device__ __forceinline__ int cslot(int s) {
  int i = s & 3;
  return i == 0 ? 124928 : (i == 1 ? 69632 : (i == 2 ? 0 : 34816));
}
__device__ __forceinline__ unsigned pk(float a, float b) {
  f32x2 v = {a, b};
  return __builtin_bit_cast(unsigned, __builtin_convertvector(v, bf16x2_t));
}
__device__ __forceinline__ float blo(unsigned u) { return __uint_as_float(u << 16); }
__device__ __forceinline__ float bhi(unsigned u) { return __uint_as_float(u & 0xffff0000u); }
__device__ __forceinline__ float rbf(float a) { return blo(pk(a, 0.f)); }
__device__ __forceinline__ bf16x8 ld128(AS3 char* p) { return *(AS3 bf16x8*)p; }
__device__ __forceinline__ bf16x8 ldtr(AS3 char* p) {
  s16x4 lo = __builtin_amdgcn_ds_read_tr16_b64_v4i16((AS3 s16x4*)p);
  s16x4 hi = __builtin_amdgcn_ds_read_tr16_b64_v4i16((AS3 s16x4*)(p + 4608));
  return __builtin_shufflevector(lo, hi, 0, 1, 2, 3, 4, 5, 6, 7);
}
#define MFMA(a, b, c) __builtin_amdgcn_mfma_f32_16x16x32_bf16(a, b, c, 0, 0, 0)
#define MFMA_ACC(c, a, b) asm volatile("v_mfma_f32_16x16x32_bf16 %0, %1, %2, %0" : "+v"(c) : "v"(a), "v"(b))
#define MFMA_NEW(c, a, b) asm volatile("v_mfma_f32_16x16x32_bf16 %0, %1, %2, 0" : "=&v"(c) : "v"(a), "v"(b))
#define MFMA_Z(c, a, b) do { if (Z) MFMA_NEW(c, a, b); else MFMA_ACC(c, a, b); } while (0)
#define NOP_MFMA_READ() do { SCHED(); asm volatile("s_nop 7\n\ts_nop 3"); SCHED(); } while (0)
#define NOP_VALU_WRITE() do { SCHED(); asm volatile("s_nop 3"); SCHED(); } while (0)
#define BAR() do { asm volatile("" ::: "memory"); __builtin_amdgcn_s_barrier(); asm volatile("" ::: "memory"); } while (0)
#define SCHED() __builtin_amdgcn_sched_barrier(0)
#define WAIT_LGKM0() __builtin_amdgcn_s_waitcnt(0xC07F)
template <int N> __device__ __forceinline__ void wait_vm() {
  asm volatile("s_waitcnt vmcnt(%0)" ::"n"(N) : "memory");
}
template <int AUX = 0>
__device__ __forceinline__ void dma(rsrc_t r, AS3 char* l, int voff, int soff) {
  __builtin_amdgcn_raw_ptr_buffer_load_lds(r, (AS3 void*)l, 16, voff, soff, 0, AUX);
}
template <int NT>
__device__ __forceinline__ void touch(const void* base, int off, unsigned lds_sink) {
  unsigned keep;
  if (NT)
    asm volatile("s_mov_b32 %0, m0\n\ts_mov_b32 m0, %3\n\ts_nop 0\n\tglobal_load_lds_dword %1, %2 nt\n\ts_mov_b32 m0, %0"
                 : "=&s"(keep) : "v"(off), "s"(base), "s"(lds_sink) : "memory");
  else
    asm volatile("s_mov_b32 %0, m0\n\ts_mov_b32 m0, %3\n\ts_nop 0\n\tglobal_load_lds_dword %1, %2\n\ts_mov_b32 m0, %0"
                 : "=&s"(keep) : "v"(off), "s"(base), "s"(lds_sink) : "memory");
}
template <int NT>
__device__ __forceinline__ void touch(const void* base, int, unsigned lds_sink, int off) {
  unsigned keep;
  asm volatile("s_mov_b32 %0, m0\n\ts_mov_b32 m0, %2\n\ts_nop 0\n\tglobal_load_lds_dword %1, off\n\ts_mov_b32 m0, %0"
               : "=&s"(keep) : "v"((const char*)base + off), "s"(lds_sink) : "memory");
}
__device__ __forceinline__ u32x4 swap_pair(const f32x4& a, const f32x4& b) {
  unsigned x0 = pk(a[0], a[1]), x1 = pk(a[2], a[3]), y0 = pk(b[0], b[1]), y1 = pk(b[2], b[3]);
  auto s0 = __builtin_amdgcn_permlane16_swap(x0, y0, false, false);
  auto s1 = __builtin_amdgcn_permlane16_swap(x1, y1, false, false);
  u32x4 v = {s0[0], s1[0], s0[1], s1[1]};
  return v;
}
__device__ __forceinline__ unsigned relu2(unsigned v) {
  typedef __attribute__((ext_vector_type(2))) short s16x2;
  return __builtin_bit_cast(unsigned, __builtin_elementwise_max(__builtin_bit_cast(s16x2, v), s16x2{0, 0}));
}
__device__ __forceinline__ u32x4 relu4(u32x4 v) { return u32x4{relu2(v[0]), relu2(v[1]), relu2(v[2]), relu2(v[3])}; }
__device__ __forceinline__ unsigned poll_issue(const AS1 unsigned* p) {
  unsigned f;
  asm volatile("global_load_dword %0, %1, off sc1" : "=v"(f) : "v"(p) : "memory");
  return f;
}
__device__ __forceinline__ void poll_check(unsigned f, const AS1 unsigned* p, unsigned tag) {
  asm volatile("" : "+v"(f));
  unsigned spins = 0;
#pragma unroll 1
  while (!__all(f == tag)) {
    __builtin_amdgcn_s_sleep(2);
    asm volatile("global_load_dword %0, %1, off sc1\n\ts_waitcnt vmcnt(0)" : "=&v"(f) : "v"(p) : "memory");
    if (++spins > SPIN_MAX) break;
  }
}
__device__ __forceinline__ void flag_store(AS1 unsigned* p, unsigned v) {
  __hip_atomic_store(p, v, __ATOMIC_RELAXED, __HIP_MEMORY_SCOPE_AGENT);
}

#define RSTAMP(i) do {} while (0)
#define STAMP(i) do {} while (0)

__device__ __forceinline__ int rot9(int st, int k) { const int t = st + k; return t >= 9 ? t - 9 : t; }
__device__ __forceinline__ int rot4(int st, int k, int lim) { return st < lim ? (st & ~3) | ((st + k) & 3) : st; }
struct Ids { uint64_t pc0; int wg, rho, sig, loc, rb64, pc, rb256, j, rb128, cb, ra; };

#define LBAR() BAR()
template <int NB>
__device__ __forceinline__ void loader(const Params& P, const Ids& I, AS3 char* L, int lw, int lane, unsigned tag) {
  const int wg = I.wg, wave = lw + 4;
  (void)wg; (void)wave;
  constexpr int N1 = 2 + NB, N2 = 8 + NB, N3 = 4 + NB;
  const rsrc_t rx = mk(P.x, S * D * 2);
  const rsrc_t rwd = mk((I.pc >> 2) ? P.wdi : P.wdr, D * D * 2);
  const rsrc_t rw1 = mk(P.w1, D * F * 2);
  const rsrc_t rw2 = mk(P.w2, F * D * 2);
  const rsrc_t rs = mk((I.sig >> 1) ? P.wsi : P.wsr, S * S * 2);
  const rsrc_t rws = mk(P.ws, WS_END);
  const int sw = ((lane & 7) ^ (((lw & 1) << 2) + (lane >> 4))) << 4;
  const int vx = (lw * 8 + (lane >> 3)) * (D * 2) + sw;
  const int vs = (lw * 8 + (lane >> 3)) * (S * 2) + sw;
  const int vl = lane * 16;
  int v1[NB], v2[NB];
#pragma unroll
  for (int i = 0; i < NB; ++i) {
    int c = (lw + 4 * i) * 64 + lane, r = c / 18, col = c - r * 18;
    int k = (r & 32) + ((r >> 2) & 3) * 8 + ((r >> 4) & 1) * 4 + (r & 3);
    v1[i] = k * (F * 2) + col * 16;
    v2[i] = k * (D * 2) + col * 16;
  }
  auto issueAx = [&](int st) {
    AS3 char* b = L + (st & 3) * HS + lw * 1024;
#pragma unroll
    for (int i = 0; i < 2; ++i) dma(rx, b + i * 4096, vx, (I.rb64 * 64 + i * 32) * (D * 2) + rot9(st, I.ra) * 128);
  };
  auto issueAw = [&](int st) {
    AS3 char* b = L + (st & 3) * HS + lw * 1024;
#pragma unroll
    for (int i = 0; i < NB; ++i) dma<AUX_WD>(rwd, b + 8192 + i * 4096, vx, ((I.pc & 3) * 144 + i * 32) * (D * 2) + rot9(st, I.ra) * 128);
  };
  auto issueA = [&](int st) { issueAx(st); issueAw(st); };
  auto issueBw = [&](int st) {
    AS3 char* b = L + (2 - st % 3) * BS + lw * 1024;
#pragma unroll
    for (int i = 0; i < NB; ++i) dma<AUX_W1>(rw1, b + 32768 + i * 4096, v1[i], rot4(st, ROT_B * I.rb256, 8) * 64 * (F * 2) + (I.sig * 8 + I.j) * 288);
  };
  auto issueBx = [&](int st) {
    AS3 char* b = L + (2 - st % 3) * BS + lw * 1024;
#pragma unroll
    for (int i = 0; i < 8; ++i) dma(rx, b + i * 4096, vx, (I.rb256 * 256 + i * 32) * (D * 2) + rot4(st, ROT_B * I.rb256, 8) * 128);
  };
  auto issueB = [&](int st) { issueBw(st); issueBx(st); };
  auto issueC = [&](int st) {
    AS3 char* b = L + cslot(st) + lw * 1024;
#pragma unroll
    for (int i = 0; i < 4; ++i) {
      const int so = (I.rb128 * 128 + i * 32) * (S * 2) + (I.sig & 1) * 2048 + rot4(st, ROT_C * I.cb, 16) * 128;
      dma<AUX_WS>(rs, b + i * 4096, vs, so);
    }
#pragma unroll
    for (int i = 0; i < NB; ++i) dma<AUX_SC1>(rws, b + 16384 + i * 4096, vl, WS_AB + ((I.sig * 16 + rot4(st, ROT_C * I.cb, 16)) * 4 + I.cb) * BIMG + (lw + 4 * i) * 1024);
  };
  auto issueD = [&](int st) {
    AS3 char* b = L + cslot(st) + lw * 1024;
    const int t = ROT_D == 2 ? (st < 32 ? ((st - 16) & 8) | ((st + I.rb128) & 7) : st - 16) : rot4(st - 16, ROT_D * I.rb128, 16);
#pragma unroll
    for (int i = 0; i < NB; ++i) dma<AUX_W2>(rw2, b + 16384 + i * 4096, v2[i], (I.sig * 1152 + t * 64) * (D * 2) + I.cb * 288);
#pragma unroll
    for (int i = 0; i < 4; ++i)
      dma<AUX_SC1>(rws, b + i * 4096, vl, WS_H + ((I.rb128 * 8 + 2 * i + (lw >> 1)) * 144 + I.sig * 36 + 2 * t + (lw & 1)) * 1024);
  };
  AS1 unsigned* wsu = (AS1 unsigned*)P.ws;

  const unsigned sink = (unsigned)(uintptr_t)(L + LDS_PF) + lw * 256;
  auto pfW1 = [&](int st) {
    const int l = min(lane, 35), hi = l >= 18;
    if (PF_W1) touch<NT_W1>(P.w1, (st * 64 + I.loc * 2 + hi) * (F * 2) + I.sig * 2304 + (l - hi * 18) * 128, sink);
  };
  auto pfWs = [&](int st) {
    if (PF_WS) touch<NT_WS>((I.sig >> 1) ? P.wsi : P.wsr,
                            (I.rho * 1024 + I.loc * 32 + (lane >> 1)) * (S * 2) + (I.sig & 1) * 2048 + (st + (lane & 1)) * 128, sink);
  };
  auto pfW2 = [&](int t) {
    if (PF_W2) touch<NT_W2>(P.w2, (I.sig * 1152 + t * 64) * (D * 2) + (I.loc * 54 + min(lane, 53)) * 128, sink);
  };
  auto pf = [&](int step) {
    if (NB == 4) {
      if (lw == 2) {
        const int k1 = (step - W1_0) / W1_S, k2 = (step - W2_0) / W2_S;
        if (step >= W1_0 && k1 * W1_S == step - W1_0 && k1 < 9) pfW1(k1);
        if (step >= W2_0 && k2 * W2_S == step - W2_0 && k2 < 6) pfW2(k2 * 3);
      } else {
        const int k = (step - WS_0) / WS_S;
        if (step >= WS_0 && k * WS_S == step - WS_0 && k * 2 < PF_WSN) pfWs(k * 2);
      }
    }
  };
  auto arm = [&](int i) {
    const u32x4 pz = {POISON, POISON, POISON, POISON};
    __builtin_amdgcn_raw_buffer_store_b128(pz, rws, WS_XP + I.wg * 27648 + min(lw * 7 + i, 26) * 1024 + vl, 0, AUX_SC1);
  };
  STAMP(0); RSTAMP(13);
  if (lw >= 2) dma(mk(P.gw, D * 2), L + LDS_GW + (lw - 2) * 1024, vl + (lw - 2) * 1024, 0);
  int bi = 0; (void)bi;
  issueA(0); issueA(1); issueA(2);
  asm volatile("" ::"s"(tag));
  wait_vm<2 * N1>(); LBAR(); issueA(3); pf(0);
  STAMP(1);
#pragma unroll 1
  for (int k = 0; k < 5; ++k) { wait_vm<2 * N1>(); LBAR(); issueA(k + 4); pf(k + 1); }
  wait_vm<2 * N1>(); LBAR(); issueBw(0); pf(6);
  wait_vm<N1 + NB>(); LBAR(); issueBx(0); pf(7);
  wait_vm<N2>(); LBAR(); issueBw(1); pf(8);
  LBAR(); issueBx(1); pf(9);
  STAMP(2);
  wait_vm<N2>(); LBAR(); issueB(2); pf(10);
  const AS1 unsigned* pfa = wsu + WS_FA / 4 + I.cb * 64 + I.sig * 16 + (lane & 15);
  const AS1 unsigned long long* pg = (const AS1 unsigned long long*)(wsu + WS_G / 4) + I.rb256 * 256 + lw * 64 + lane;
  unsigned f = 0;
  u32x2 gg = {0u, 0u};
#pragma unroll 1
  for (int st = 3; st < 9; ++st) {
    wait_vm<N2>(); LBAR();
    if (st == 5 && lw == 0 && lane == 0) flag_store(wsu + WS_FA / 4 + (I.pc & 3) * 64 + (I.pc >> 2) * 32 + I.rb64, tag);
    if (st == 8) {
      f = poll_issue(pfa);
      asm volatile("global_load_dwordx2 %0, %1, off sc1" : "=v"(gg) : "v"(pg) : "memory");
    }
    arm(st - 3);
    issueB(st);
    pf(st + 8);
  }
  wait_vm<N2>(); LBAR();
  poll_check(f, pfa, tag);
  arm(6); issueC(0); pf(17);
  {
    asm volatile("" : "+v"(gg));
    unsigned spins = 0;
#pragma unroll 1
    while (!__all(gg[1] == tag)) {
      __builtin_amdgcn_s_sleep(2);
      asm volatile("global_load_dwordx2 %0, %1, off sc1\n\ts_waitcnt vmcnt(0)" : "=&v"(gg) : "v"(pg) : "memory");
      if (++spins > SPIN_MAX) break;
    }
    asm volatile("ds_write_b32 %0, %1\n\ts_waitcnt lgkmcnt(0)" ::"v"((unsigned)(uintptr_t)(L + LDS_G + (lw * 64 + lane) * 4)), "v"(gg[0]) : "memory");
  }
  wait_vm<N3>(); LBAR(); issueC(1); pf(18);
  LBAR(); issueC(2); issueC(3); pf(19);
  STAMP(3);
  wait_vm<0>(); LBAR(); pf(20);
  const AS1 unsigned* pfh = wsu + WS_FH / 4 + I.rb256 * 32 + ((lane & 8) ? (lane & 3) * 8 + I.j : I.sig * 8 + (lane & 7));
#pragma unroll 1
  for (int u = 0; u < 12; ++u) {
    wait_vm<2 * N3>(); LBAR();
    if (u == 8 && lw == 0 && lane == 0) flag_store(wsu + WS_FH / 4 + I.rb256 * 32 + I.sig * 8 + I.j, tag);
    if (u == 10) f = poll_issue(pfh);
    issueC(u + 4); pf(21 + u);
  }
  wait_vm<2 * N3>(); LBAR();
  STAMP(4);
  poll_check(f, pfh, tag);
  STAMP(5);
  issueD(16); pf(33);
#pragma unroll 1
  for (int u = 13; u < 30; ++u) {
    wait_vm<2 * N3>(); LBAR();
    issueD(u + 4); pf(21 + u);
  }
  wait_vm<2 * N3>(); LBAR();
  wait_vm<N3>(); LBAR();
  wait_vm<0>(); LBAR();
  LBAR();
  STAMP(6);
  if (I.wg == 0 && lw == 1) {
    const AS1 unsigned* pa = wsu + WS_FA / 4 + lane;
#pragma unroll 1
    for (int i = 0; i < 4; ++i) poll_check(0u - (tag == 0u), pa + i * 64, tag);
    if (lane == 0) flag_store(wsu + WS_EPOCH / 4, tag);
  }
  BAR();
  BAR();
  STAMP(7);
}

template <bool Z = false>
__device__ __forceinline__ void unit(f32x4 (&acc)[4][9], const bf16x8 (&Ac)[4], bf16x8 (&An)[4], bf16x8 (&B)[7],
                                     const bf16x8 (&Xc)[2], bf16x8 (&Xn)[2], AS3 char* a, AS3 char* a2, AS3 char* b) {
  SCHED();
#pragma unroll
  for (int n = 0; n < 7; ++n) {
    MFMA_Z(acc[0][n], B[n], Ac[0]);
    SCHED();
    if (n < 4) An[n] = ld128((n < 2 ? a : a2) + (n & 1) * 2048);
    SCHED();
    MFMA_Z(acc[1][n], B[n], Ac[1]);
    SCHED();
    if (n < 2) Xn[n] = ldtr(b + (7 + n) * 32);
    SCHED();
    MFMA_Z(acc[2][n], B[n], Ac[2]);
    MFMA_Z(acc[3][n], B[n], Ac[3]);
    SCHED();
    B[n] = ldtr(b + n * 32);
    SCHED();
  }
#pragma unroll
  for (int n = 0; n < 2; ++n)
#pragma unroll
    for (int m = 0; m < 4; ++m) MFMA_Z(acc[m][7 + n], Xc[n], Ac[m]);
  SCHED();
  WAIT_LGKM0();
}
__device__ __forceinline__ void first_read(bf16x8 (&A)[4], bf16x8 (&B)[7], bf16x8 (&X)[2], AS3 char* a, AS3 char* a2, AS3 char* b) {
#pragma unroll
  for (int m = 0; m < 4; ++m) A[m] = ld128((m < 2 ? a : a2) + (m & 1) * 2048);
#pragma unroll
  for (int n = 0; n < 7; ++n) B[n] = ldtr(b + n * 32);
#pragma unroll
  for (int n = 0; n < 2; ++n) X[n] = ldtr(b + (7 + n) * 32);
  WAIT_LGKM0();
}
template <int ODD>
__device__ __forceinline__ void store_h(const f32x4 (&acc)[4][9], const float (&om)[4], rsrc_t rws, int sbase, int r, int q) {
  const int vp = ((((q & 1) << 1) | (q >> 1)) * 16 + r) * 16 + sbase + ODD * 1024;
  const int vh = ((ODD * 2 + (q >> 1)) * 16 + r) * 16 + (q & 1) * 8 + sbase + (ODD ? 0 : 4096);
#pragma unroll
  for (int m = 0; m < 4; ++m) {
    f32x4 t[9];
#pragma unroll
    for (int n = 0; n < 9; ++n)
#pragma unroll
      for (int e = 0; e < 4; ++e) t[n][e] = acc[m][n][e] * om[m];
#pragma unroll
    for (int p = 0; p < 4; ++p)
      __builtin_amdgcn_raw_buffer_store_b128(relu4(swap_pair(t[2 * p + ODD], t[2 * p + 1 + ODD])), rws,
                                             vp + m * 147456 + p * 1024, 0, AUX_SC1);
    const f32x4 s = t[ODD ? 0 : 8];
    u32x2 v = {relu2(pk(s[0], s[1])), relu2(pk(s[2], s[3]))};
    __builtin_amdgcn_raw_buffer_store_b64(v, rws, vh + m * 147456, 0, AUX_SC1);
  }
}

__device__ __forceinline__ void consumer(const Params& P, const Ids& I, AS3 char* L, int cw, int lane, unsigned tag) {
  const int wg = I.wg, wave = cw;
  (void)wg; (void)wave;
  const int r = lane & 15, q = lane >> 4;
  const rsrc_t rws = mk(P.ws, WS_END);
  STAMP(0);
  asm volatile("" ::"s"(tag));
  const unsigned sink = (unsigned)(uintptr_t)(L + LDS_PF) + cw * 256;
  if (PF_X && cw == 0) touch<0>(P.x, (I.wg * 72 + lane) * 128, sink);
  if (PF_X && cw == 1)
    touch<0>(lane < 8 ? P.x : (I.wg < 128 ? P.wdr : P.wdi), 0, sink, lane < 8 ? (I.wg * 72 + 64 + lane) * 128 : min((I.wg & 127) * 41 + lane - 8, 5183) * 128);
  const int aL0 = r * 128 + ((q ^ ((r >> 1) & 7)) << 4), aL1 = aL0 ^ 64;
  const int bL = (4 * q + ((lane >> 2) & 3)) * 288 + (lane & 3) * 8;
  f32x4 acc[4][9];
  {
    f32x4 lg = {0.f, 0.f, 0.f, 0.f};
#pragma unroll
    for (int n = 0; n < 9; ++n) { acc[0][n] = f32x4{0.f, 0.f, 0.f, 0.f}; asm volatile("" : "+v"(acc[0][n])); }
    asm volatile("" : "+v"(lg));
#pragma unroll
    for (int m = 1; m < 4; ++m)
#pragma unroll
      for (int n = 0; n < 9; ++n) { acc[m][n] = f32x4{0.f, 0.f, 0.f, 0.f}; asm volatile("" : "+v"(acc[m][n])); }
    bf16x8 xa, xb, w0[10], w1[10];
    BAR();
    STAMP(1);
    xa = ld128(L + aL0 + cw * 2048); xb = ld128(L + aL1 + cw * 2048);
#pragma unroll
    for (int n = 0; n < 9; ++n) { w0[n] = ld128(L + aL0 + 8192 + n * 2048); w1[n] = ld128(L + aL1 + 8192 + n * 2048); }
    w0[9] = ld128(L + LDS_GW + I.ra * 128 + q * 16); w1[9] = ld128(L + LDS_GW + I.ra * 128 + q * 16 + 64);
    WAIT_LGKM0();
    NOP_VALU_WRITE();
    int hs = HS;
#pragma unroll 1
    for (int st = 0; st < 9; ++st) {
      BAR();
      AS3 char* b0 = L + hs + aL0;
      AS3 char* b1 = L + hs + aL1;
      AS3 char* bg = L + LDS_GW + rot9(st + 1, I.ra) * 128 + q * 16;
      SCHED();
#pragma unroll
      for (int n = 0; n < 9; ++n) { MFMA_ACC(acc[0][n], w0[n], xa); SCHED(); w0[n] = ld128(b0 + 8192 + n * 2048); SCHED(); }
      MFMA_ACC(lg, w0[9], xa); SCHED(); w0[9] = ld128(bg); xa = ld128(b0 + cw * 2048); SCHED();
#pragma unroll
      for (int n = 0; n < 9; ++n) { MFMA_ACC(acc[0][n], w1[n], xb); SCHED(); w1[n] = ld128(b1 + 8192 + n * 2048); SCHED(); }
      MFMA_ACC(lg, w1[9], xb); SCHED(); w1[9] = ld128(bg + 64); xb = ld128(b1 + cw * 2048); SCHED();
      WAIT_LGKM0();
      hs = (hs == 3 * HS) ? 0 : hs + HS;
    }
    NOP_MFMA_READ();
    STAMP(2);
    if (q == 0 && (r >> 1) == I.pc) {
      const float g = rbf(1.f / (1.f + __expf(-rbf(lg[0]))));
      __hip_atomic_store((AS1 unsigned long long*)((AS1 char*)P.ws + WS_G) + I.rb64 * 64 + cw * 16 + r,
                         ((unsigned long long)tag << 32) | __float_as_uint(g), __ATOMIC_RELAXED, __HIP_MEMORY_SCOPE_AGENT);
    }
    const float sg = (I.pc >> 2) ? -1.f : 1.f;
    const int sl = cw * 16 + r, k = sl & 31;
    const int rho = (sl & 32) + ((k >> 2) & 1) * 16 + (k >> 3) * 4 + (k & 3);
    const int sb = WS_AB + (((I.pc >> 2) * 32 + I.rb64) * 4 + (I.pc & 3)) * BIMG;
    const int vp = sb + rho * 288 + (((q & 1) << 1) | (q >> 1)) * 16;
#pragma unroll
    for (int p = 0; p < 4; ++p)
      __builtin_amdgcn_raw_buffer_store_b128(swap_pair(acc[0][2 * p] * sg, acc[0][2 * p + 1] * sg), rws, vp + p * 64, 0, AUX_SC1);
    const f32x4 s = acc[0][8] * sg;
    u32x2 v = {pk(s[0], s[1]), pk(s[2], s[3])};
    __builtin_amdgcn_raw_buffer_store_b64(v, rws, sb + rho * 288 + 256 + q * 8, 0, AUX_SC1);
  }
  const int mh = cw >> 1, kg = cw & 1;
  bf16x8 A0[4], A1[4], B[7], X0[2], X1[2];
  {
#pragma unroll
    for (int n = 0; n < 9; ++n) { acc[0][n] = f32x4{0.f, 0.f, 0.f, 0.f}; asm volatile("" : "+v"(acc[0][n])); }
    const int a0 = aL0 + cw * 8192, a1 = aL1 + cw * 8192, b0 = bL + 32768;
    BAR();
    STAMP(3);
    int sl = 2 * BS;
    first_read(A0, B, X0, L + sl + a0, L + sl + a0 + 4096, L + sl + b0);
    NOP_VALU_WRITE();
#pragma unroll 1
    for (int st = 0; st < 9; ++st) {
      if (st == 2) wait_vm<0>();
      unit(acc, A0, A1, B, X0, X1, L + sl + a1, L + sl + a1 + 4096, L + sl + b0 + 9216);
      BAR();
      sl = (sl == 0) ? 2 * BS : sl - BS;
      unit(acc, A1, A0, B, X1, X0, L + sl + a0, L + sl + a0 + 4096, L + sl + b0);
    }
    NOP_MFMA_READ();
    STAMP(4);
    float om[4];
#pragma unroll
    for (int m = 0; m < 4; ++m) {
      om[m] = rbf(1.f - *(AS3 float*)(L + LDS_G + (cw * 64 + m * 16 + r) * 4));
    }
    const int sb = WS_H + ((I.rb256 * 16 + cw * 4) * 144 + I.sig * 36 + ((I.j * 9) >> 1)) * 1024;
    if (I.j & 1) store_h<1>(acc, om, rws, sb, r, q);
    else store_h<0>(acc, om, rws, sb, r, q);
    STAMP(5);
  }
  {
    asm volatile("" : "+v"(lane));
    const int aS = (kg ? aL1 : aL0) + mh * 8192 + kg * 4096, aH = mh * 8192 + kg * 5120 + lane * 16;
    const int d2 = kg ? -4096 : 4096;
    const int bb = bL + 16384 + kg * 9216;
    BAR();
    STAMP(6);
    first_read(A0, B, X0, L + cslot(0) + aS, L + cslot(0) + aS + d2, L + cslot(0) + bb);
    BAR();
    unit<true>(acc, A0, A1, B, X0, X1, L + cslot(1) + aS, L + cslot(1) + aS + d2, L + cslot(1) + bb);
    BAR();
    unit(acc, A1, A0, B, X1, X0, L + cslot(2) + aS, L + cslot(2) + aS + d2, L + cslot(2) + bb);
#pragma unroll 1
    for (int u = 2; u < 16; u += 2) {
      if (u == 8) wait_vm<0>();
      BAR();
      const int s1 = cslot(u + 1), s2 = cslot(u + 2), an = u < 14 ? aS : aH;
      unit(acc, A0, A1, B, X0, X1, L + s1 + aS, L + s1 + aS + d2, L + s1 + bb);
      BAR();
      unit(acc, A1, A0, B, X1, X0, L + s2 + an, L + s2 + an + d2, L + s2 + bb);
    }
    NOP_MFMA_READ();
    STAMP(7);
#pragma unroll
    for (int m = 0; m < 4; ++m) {
      const float g = *(AS3 float*)(L + LDS_G + ((I.j & 1) * 128 + mh * 64 + (m ^ (2 * kg)) * 16 + (lane & 15)) * 4);
#pragma unroll
      for (int n = 0; n < 9; ++n) { acc[m][n] *= g; asm volatile("" : "+v"(acc[m][n])); }
    }
    NOP_VALU_WRITE();
#pragma unroll 1
    for (int u = 16; u < 34; u += 2) {
      BAR();
      const int s1 = cslot(u + 1), s2 = cslot(u + 2);
      unit(acc, A0, A1, B, X0, X1, L + s1 + aH, L + s1 + aH + d2, L + s1 + bb);
      BAR();
      unit(acc, A1, A0, B, X1, X0, L + s2 + aH, L + s2 + aH + d2, L + s2 + bb);
    }
    NOP_MFMA_READ();
    STAMP(8);
  }
  {
    asm volatile("" : "+v"(lane));
    AS3 char* wr = L + cw * BIMG + lane * 16;
    AS3 char* rd = L + (cw ^ 1) * BIMG + lane * 16;
#pragma unroll
    for (int m = 0; m < 2; ++m)
#pragma unroll
      for (int n = 0; n < 9; ++n) *(AS3 f32x4*)(wr + (m * 9 + n) * 1024) = acc[2 + m][n];
    WAIT_LGKM0();
    BAR();
    STAMP(10);
#pragma unroll
    for (int m = 0; m < 2; ++m)
#pragma unroll
      for (int n = 0; n < 9; ++n) acc[m][n] += *(AS3 f32x4*)(rd + (m * 9 + n) * 1024);
    if (cw == I.sig) {
      AS3 char* kp = L + 4 * BIMG + lane * 16;
#pragma unroll
      for (int m = 0; m < 2; ++m) {
#pragma unroll
        for (int n = 0; n < 8; ++n) *(AS3 f32x4*)(kp + (m * 8 + n) * 1024) = acc[m][n];
        *(AS3 f32x4*)(kp + (16 + m) * 1024) = acc[m][8];
      }
      WAIT_LGKM0();
    } else {
      const int sb = WS_XP + ((I.loc * 8 + I.rho * 4 + cw) * 3 + ((I.sig - cw - 1) & 3)) * 9216 + lane * 16;
#pragma unroll
      for (int it = 0; it < 9; ++it) {
        const f32x4 a = it < 8 ? acc[it >> 2][(it & 3) * 2] : acc[0][8];
        const f32x4 b = it < 8 ? acc[it >> 2][(it & 3) * 2 + 1] : acc[1][8];
        u32x4 v = {min(pk(a[0], a[1]), POISON - 1), min(pk(a[2], a[3]), POISON - 1), min(pk(b[0], b[1]), POISON - 1), min(pk(b[2], b[3]), POISON - 1)};
        __builtin_amdgcn_raw_buffer_store_b128(v, rws, sb + it * 1024, 0, AUX_SC1);
      }
    }
    STAMP(9);
    BAR();
  }
}

__device__ __forceinline__ void final_items(const Params& P, const Ids& I, AS3 char* L, int w, int lane) {
  const rsrc_t rws = mk(P.ws, WS_END);
  const rsrc_t ro = mk(P.out, S * D * 2);
  const int r = lane & 15, q = lane >> 4;
  const int vb = WS_XP + I.wg * 27648 + lane * 16;
  f32x4 a = *(AS3 f32x4*)(L + 4 * BIMG + w * 2048 + lane * 16);
  f32x4 b = *(AS3 f32x4*)(L + 4 * BIMG + w * 2048 + 1024 + lane * 16);
  f32x4 c = *(AS3 f32x4*)(L + 4 * BIMG + 16384 + (w & 1) * 1024 + lane * 16);
  u32x4 p[3];
  u32x2 e[3];
#pragma unroll
  for (int i = 0; i < 3; ++i) { e[i] = u32x2{0u, 0u}; p[i] = u32x4{0u, 0u, 0u, 0u}; }
  unsigned todo = 0x7;
#pragma unroll 1
  for (unsigned spins = 0; todo && spins < SPIN_MAX; ++spins) {
#pragma unroll
    for (int i = 0; i < 3; ++i)
      if (todo >> i & 1) {
        p[i] = __builtin_amdgcn_raw_buffer_load_b128(rws, vb + i * 9216 + w * 1024, 0, AUX_SC1);
        if (w < 2) e[i] = __builtin_amdgcn_raw_buffer_load_b64(rws, vb + i * 9216 + 8192 + w * 8, 0, AUX_SC1);
      }
#pragma unroll
    for (int i = 0; i < 3; ++i)
      if (todo >> i & 1) {
        const unsigned mx = max(max(max(p[i][0], p[i][1]), max(p[i][2], p[i][3])), max(e[i][0], e[i][1]));
        if (__all(mx != POISON)) todo &= ~(1u << i);
      }
    if (todo) __builtin_amdgcn_s_sleep(POLL_SLEEP);
  }
#pragma unroll
  for (int i = 0; i < 3; ++i) {
    a += f32x4{blo(p[i][0]), bhi(p[i][0]), blo(p[i][1]), bhi(p[i][1])};
    b += f32x4{blo(p[i][2]), bhi(p[i][2]), blo(p[i][3]), bhi(p[i][3])};
    c += f32x4{blo(e[i][0]), bhi(e[i][0]), blo(e[i][1]), bhi(e[i][1])};
  }
  const int row0 = I.rb128 * 128 + I.sig * 32, col0 = I.cb * 144;
  __builtin_amdgcn_raw_buffer_store_b128(swap_pair(a, b), ro, (row0 + (w >> 2) * 16 + r) * (D * 2) + (col0 + (w & 3) * 32) * 2 + (((q & 1) << 1) | (q >> 1)) * 16, 0, 0);
  if (w < 2) {
    u32x2 v = {pk(c[0], c[1]), pk(c[2], c[3])};
    __builtin_amdgcn_raw_buffer_store_b64(v, ro, (row0 + w * 16 + r) * (D * 2) + (col0 + 128) * 2 + q * 8, 0, 0);
  }
}
}

__global__ __launch_bounds__(512, 2) void gated_mix_fnet_ffn_kernel(Params P) {
  __shared__ __attribute__((aligned(1024))) char lds[LDS_BYTES];
  AS3 char* L = (AS3 char*)lds;
  const int lane = threadIdx.x & 63;
  const int wave = __builtin_amdgcn_readfirstlane(threadIdx.x >> 6);
  Ids I;
  asm volatile("s_getpc_b64 %0" : "=s"(I.pc0));
  I.wg = blockIdx.x;
  if (wave == 0)
    touch<0>((const void*)I.pc0, min(((I.wg >> 3) * PF_CL + (lane & (PF_CL - 1))) % 240 * 128, CODE_PF_BYTES), (unsigned)(uintptr_t)(L + LDS_PF));
  I.rho = (I.wg >> 2) & 1; I.sig = I.wg & 3; I.loc = I.wg >> 3;
  I.rb64 = 16 * I.rho + (I.loc >> 1); I.pc = 2 * I.sig + (I.loc & 1);
  I.rb256 = 4 * I.rho + (I.loc >> 3); I.j = I.loc & 7;
  I.rb128 = 2 * I.rb256 + (I.j & 1); I.cb = I.j >> 1;
  I.ra = ROT_A * ((I.rb64 & 15) % 9);
  const unsigned tag = *(const __attribute__((address_space(4))) unsigned*)((uintptr_t)P.ws + WS_EPOCH) + 1u;
  if (wave < 4) consumer(P, I, L, wave, lane, tag);
  else if (wave < 6) loader<5>(P, I, L, wave - 4, lane, tag);
  else loader<4>(P, I, L, wave - 4, lane, tag);
  {
    int tz = threadIdx.x;
    asm volatile("" : "+v"(tz));
    const int w2 = __builtin_amdgcn_readfirstlane(tz >> 6);
    __builtin_amdgcn_s_sleep(POLL_FIRST);
    final_items(P, I, L, w2, tz & 63);
  }
}

extern "C" void kernel_launch(void* const* d_in, const int* in_sizes, int n_in, void* d_out, int out_size, void* d_ws,
                              size_t ws_size, hipStream_t stream) {
  Params P;
  P.x = d_in[0]; P.wsr = d_in[1]; P.wsi = d_in[2]; P.wdr = d_in[3]; P.wdi = d_in[4];
  P.w1 = d_in[6]; P.w2 = d_in[7]; P.gw = d_in[8]; P.out = d_out; P.ws = d_ws;
  gated_mix_fnet_ffn_kernel<<<256, 512, 0, stream>>>(P);
}
```

```cpp
#include <hip/hip_runtime.h>
#include <hip/hip_bf16.h>
#include <stdint.h>

typedef __attribute__((ext_vector_type(8))) short bf16x8;
typedef __attribute__((ext_vector_type(4))) short s16x4;
typedef __attribute__((ext_vector_type(4))) float f32x4;
typedef __attribute__((ext_vector_type(2))) float f32x2;
typedef __attribute__((ext_vector_type(4))) unsigned u32x4;
typedef __attribute__((ext_vector_type(2))) unsigned u32x2;
typedef __attribute__((ext_vector_type(2))) __bf16 bf16x2_t;
#define AS3 __attribute__((address_space(3)))
#define AS1 __attribute__((address_space(1)))
typedef __amdgpu_buffer_rsrc_t rsrc_t;

namespace {
constexpr int S = 2048, D = 576, F = 4608;
constexpr int WS_EPOCH = 0;
constexpr int WS_FA = 4096;
constexpr int WS_FH = 8192;
constexpr int WS_FW = 12288;
constexpr int WS_G = 32768;
constexpr int WS_PROF = 64 << 20;
constexpr int WS_W2T = 1 << 17;
constexpr int WS_AB = 1 << 20;
constexpr int WS_XP = 6 << 20;
constexpr int WS_H = 16 << 20;
constexpr int WS_END = WS_H + 128 * 144 * 1024;
constexpr int HS = 26624;
constexpr int BS = 53248;
constexpr int LDS_PF = 3 * BS;
constexpr int LDS_GW = LDS_PF + 1024;
constexpr int LDS_G = LDS_GW + 2048;
constexpr int LDS_BYTES = LDS_G + 1024;
constexpr int BIMG = 18432;
constexpr int CODE_PF_BYTES = 30208;
constexpr unsigned SPIN_MAX = 1u << 18;
constexpr int AUX_SC1 = 16;
constexpr unsigned POISON = 0xffffffffu;

constexpr int POLL_SLEEP = 2, PF_X = 1, PRIO = 1, AUX_AB = 16, AUX_H = 16, AUX_WS = 0, AUX_W1 = 0, AUX_W2 = 0, AUX_WD = 0, WS_0 = 12, PF_WSN = 8, PF_CL = 8;

struct Params {
  const void* x; const void* wsr; const void* wsi; const void* wdr; const void* wdi;
  const void* w1; const void* w2; const void* gw; void* out; void* ws;
};

__device__ __forceinline__ rsrc_t mk(const void* p, int bytes) {
  return __builtin_amdgcn_make_buffer_rsrc((void*)p, (short)0, bytes, 0x00020000);
}
__device__ __forceinline__ int cslot(int s) {
  int i = s & 3;
  return i == 0 ? 124928 : (i == 1 ? 69632 : (i == 2 ? 0 : 34816));
}
__device__ __forceinline__ unsigned pk(float a, float b) {
  f32x2 v = {a, b};
  return __builtin_bit_cast(unsigned, __builtin_convertvector(v, bf16x2_t));
}
__device__ __forceinline__ float blo(unsigned u) { return __uint_as_float(u << 16); }
__device__ __forceinline__ float bhi(unsigned u) { return __uint_as_float(u & 0xffff0000u); }
__device__ __forceinline__ float rbf(float a) { return blo(pk(a, 0.f)); }
__device__ __forceinline__ bf16x8 ld128(AS3 char* p) { return *(AS3 bf16x8*)p; }
template <int HI = 4608>
__device__ __forceinline__ bf16x8 ldtr(AS3 char* p) {
  s16x4 lo = __builtin_amdgcn_ds_read_tr16_b64_v4i16((AS3 s16x4*)p);
  s16x4 hi = __builtin_amdgcn_ds_read_tr16_b64_v4i16((AS3 s16x4*)(p + HI));
  return __builtin_shufflevector(lo, hi, 0, 1, 2, 3, 4, 5, 6, 7);
}
template <bool DENSE>
__device__ __forceinline__ bf16x8 ldb(AS3 char* b, AS3 char* bt, int n) {
  if (!DENSE) return ldtr<4608>(b + n * 32);
  if (n == 8) return ldtr<512>(bt);
  return ldtr<4096>((AS3 char*)((unsigned)(uintptr_t)b ^ (unsigned)(n << 5)));
}
#define MFMA(a, b, c) __builtin_amdgcn_mfma_f32_16x16x32_bf16(a, b, c, 0, 0, 0)
#define MFMA_ACC(c, a, b) asm volatile("v_mfma_f32_16x16x32_bf16 %0, %1, %2, %0" : "+v"(c) : "v"(a), "v"(b))
#define MFMA_NEW(c, a, b) asm volatile("v_mfma_f32_16x16x32_bf16 %0, %1, %2, 0" : "=&v"(c) : "v"(a), "v"(b))
#define MFMA_Z(c, a, b) do { if (Z) MFMA_NEW(c, a, b); else MFMA_ACC(c, a, b); } while (0)
#define NOP_MFMA_READ() do { SCHED(); asm volatile("s_nop 7\n\ts_nop 3"); SCHED(); } while (0)
#define NOP_VALU_WRITE() do { SCHED(); asm volatile("s_nop 3"); SCHED(); } while (0)
#define BAR() do { asm volatile("" ::: "memory"); __builtin_amdgcn_s_barrier(); asm volatile("" ::: "memory"); } while (0)
#define SCHED() __builtin_amdgcn_sched_barrier(0)
#define WAIT_LGKM0() __builtin_amdgcn_s_waitcnt(0xC07F)
template <int N> __device__ __forceinline__ void wait_vm() {
  asm volatile("s_waitcnt vmcnt(%0)" ::"n"(N) : "memory");
}
template <int AUX = 0>
__device__ __forceinline__ void dma(rsrc_t r, AS3 char* l, int voff, int soff) {
  __builtin_amdgcn_raw_ptr_buffer_load_lds(r, (AS3 void*)l, 16, voff, soff, 0, AUX);
}
template <int NT>
__device__ __forceinline__ void touch(const void* base, int off, unsigned lds_sink) {
  unsigned keep;
  if (NT)
    asm volatile("s_mov_b32 %0, m0\n\ts_mov_b32 m0, %3\n\ts_nop 0\n\tglobal_load_lds_dword %1, %2 nt\n\ts_mov_b32 m0, %0"
                 : "=&s"(keep) : "v"(off), "s"(base), "s"(lds_sink) : "memory");
  else
    asm volatile("s_mov_b32 %0, m0\n\ts_mov_b32 m0, %3\n\ts_nop 0\n\tglobal_load_lds_dword %1, %2\n\ts_mov_b32 m0, %0"
                 : "=&s"(keep) : "v"(off), "s"(base), "s"(lds_sink) : "memory");
}
template <int NT>
__device__ __forceinline__ void touch(const void* base, int, unsigned lds_sink, int off) {
  unsigned keep;
  asm volatile("s_mov_b32 %0, m0\n\ts_mov_b32 m0, %2\n\ts_nop 0\n\tglobal_load_lds_dword %1, off\n\ts_mov_b32 m0, %0"
               : "=&s"(keep) : "v"((const char*)base + off), "s"(lds_sink) : "memory");
}
__device__ __forceinline__ u32x4 swap_pair(const f32x4& a, const f32x4& b) {
  unsigned x0 = pk(a[0], a[1]), x1 = pk(a[2], a[3]), y0 = pk(b[0], b[1]), y1 = pk(b[2], b[3]);
  auto s0 = __builtin_amdgcn_permlane16_swap(x0, y0, false, false);
  auto s1 = __builtin_amdgcn_permlane16_swap(x1, y1, false, false);
  u32x4 v = {s0[0], s1[0], s0[1], s1[1]};
  return v;
}
__device__ __forceinline__ unsigned relu2(unsigned v) {
  typedef __attribute__((ext_vector_type(2))) short s16x2;
  return __builtin_bit_cast(unsigned, __builtin_elementwise_max(__builtin_bit_cast(s16x2, v), s16x2{0, 0}));
}
__device__ __forceinline__ u32x4 relu4(u32x4 v) { return u32x4{relu2(v[0]), relu2(v[1]), relu2(v[2]), relu2(v[3])}; }
__device__ __forceinline__ unsigned poll_issue(const AS1 unsigned* p) {
  unsigned f;
  asm volatile("global_load_dword %0, %1, off sc1" : "=v"(f) : "v"(p) : "memory");
  return f;
}
__device__ __forceinline__ void poll_check(unsigned f, const AS1 unsigned* p, unsigned tag) {
  asm volatile("" : "+v"(f));
  unsigned spins = 0;
#pragma unroll 1
  while (!__all(f == tag)) {
    __builtin_amdgcn_s_sleep(2);
    asm volatile("global_load_dword %0, %1, off sc1\n\ts_waitcnt vmcnt(0)" : "=&v"(f) : "v"(p) : "memory");
    if (++spins > SPIN_MAX) break;
  }
}
__device__ __forceinline__ void flag_store(AS1 unsigned* p, unsigned v) {
  __hip_atomic_store(p, v, __ATOMIC_RELAXED, __HIP_MEMORY_SCOPE_AGENT);
}

#define RSTAMP(i) do {} while (0)
#define STAMP(i) do {} while (0)

__device__ __forceinline__ int rot9(int st, int k) { const int t = st + k; return t >= 9 ? t - 9 : t; }
__device__ __forceinline__ int rot4(int st, int k, int lim) { return st < lim ? (st & ~3) | ((st + k) & 3) : st; }
__device__ __forceinline__ int rot8(int st, int k) { return st < 16 ? (st & 8) | ((st + k) & 7) : st; }
struct Ids { uint64_t pc0; int wg, rho, sig, loc, rb64, pc, rb256, j, rb128, cb, ra; };

#define LBAR() BAR()
template <int NB>
__device__ __forceinline__ void loader(const Params& P, const Ids& I, AS3 char* L, int lw, int lane, unsigned tag) {
  const int wg = I.wg, wave = lw + 4;
  (void)wg; (void)wave;
  constexpr int N1 = 2 + NB, N2 = 8 + NB, N3 = 4 + NB;
  const rsrc_t rx = mk(P.x, S * D * 2);
  const rsrc_t rwd = mk((I.pc >> 2) ? P.wdi : P.wdr, D * D * 2);
  const rsrc_t rw1 = mk(P.w1, D * F * 2);
  const rsrc_t rw2 = mk(P.w2, F * D * 2);
  const rsrc_t rs = mk((I.sig >> 1) ? P.wsi : P.wsr, S * S * 2);
  const rsrc_t rws = mk(P.ws, WS_END);
  const int sw = ((lane & 7) ^ (((lw & 1) << 2) + (lane >> 4))) << 4;
  const int vx = (lw * 8 + (lane >> 3)) * (D * 2) + sw;
  const int vs = (lw * 8 + (lane >> 3)) * (S * 2) + sw;
  const int vl = lane * 16;
  const int v2 = (lw * 8 + (lane >> 4)) * (D * 2) + (((lane >> 1 & 7) ^ ((lw * 4 + (lane >> 4)) & 7)) * 2 + (lane & 1)) * 16;
  int v1[NB];
#pragma unroll
  for (int i = 0; i < NB; ++i) {
    int c = (lw + 4 * i) * 64 + lane, r = c / 18, col = c - r * 18;
    int k = (r & 32) + ((r >> 2) & 3) * 8 + ((r >> 4) & 1) * 4 + (r & 3);
    v1[i] = k * (F * 2) + col * 16;
  }
  auto issueAx = [&](int st) {
    AS3 char* b = L + (st & 3) * HS + lw * 1024;
#pragma unroll
    for (int i = 0; i < 2; ++i) dma(rx, b + i * 4096, vx, (I.rb64 * 64 + i * 32) * (D * 2) + rot9(st, I.ra) * 128);
  };
  auto issueAw = [&](int st) {
    AS3 char* b = L + (st & 3) * HS + lw * 1024;
#pragma unroll
    for (int i = 0; i < NB; ++i) dma<AUX_WD>(rwd, b + 8192 + i * 4096, vx, (i < 4 ? (I.pc & 3) * 128 + i * 32 : 512 + (I.pc & 3) * 16) * (D * 2) + rot9(st, I.ra) * 128);
  };
  auto issueA = [&](int st) { issueAx(st); issueAw(st); };
  auto issueBw = [&](int st) {
    AS3 char* b = L + (2 - st % 3) * BS + lw * 1024;
#pragma unroll
    for (int i = 0; i < NB; ++i) dma<AUX_W1>(rw1, b + 32768 + i * 4096, v1[i], rot4(st, I.rb256, 8) * 64 * (F * 2) + (I.sig * 8 + I.j) * 288);
  };
  auto issueBx = [&](int st) {
    AS3 char* b = L + (2 - st % 3) * BS + lw * 1024;
#pragma unroll
    for (int i = 0; i < 8; ++i) dma(rx, b + i * 4096, vx, (I.rb256 * 256 + i * 32) * (D * 2) + rot4(st, I.rb256, 8) * 128);
  };
  auto issueB = [&](int st) { issueBw(st); issueBx(st); };
  auto issueC = [&](int st) {
    AS3 char* b = L + cslot(st) + lw * 1024;
#pragma unroll
    for (int i = 0; i < 4; ++i) {
      const int so = (I.rb128 * 128 + i * 32) * (S * 2) + (I.sig & 1) * 2048 + rot4(st, I.cb, 16) * 128;
      dma<AUX_WS>(rs, b + i * 4096, vs, so);
    }
#pragma unroll
    for (int i = 0; i < NB; ++i) dma<AUX_AB>(rws, b + 16384 + i * 4096, vl, WS_AB + ((I.sig * 16 + rot4(st, I.cb, 16)) * 4 + I.cb) * BIMG + (lw + 4 * i) * 1024);
  };
  auto issueD = [&](int st) {
    AS3 char* b = L + cslot(st) + lw * 1024;
    const int t = rot8(st - 16, I.rb128);
#pragma unroll
    for (int i = 0; i < 4; ++i)
      dma<AUX_W2>(rw2, b + 16384 + i * 4096, v2, (I.sig * 1152 + t * 64 + (i >> 1) * 32 + (i & 1) * 4) * (D * 2) + I.cb * 256);
    if (NB == 5) dma<AUX_SC1>(rws, b + 32768, vl, WS_W2T + (I.cb * F + I.sig * 1152 + t * 64) * 32 + lw * 1024);
#pragma unroll
    for (int i = 0; i < 4; ++i)
      dma<AUX_H>(rws, b + i * 4096, vl, WS_H + ((I.rb128 * 8 + 2 * i + (lw >> 1)) * 144 + I.sig * 36 + 2 * t + (lw & 1)) * 1024);
  };
  AS1 unsigned* wsu = (AS1 unsigned*)P.ws;

  const unsigned sink = (unsigned)(uintptr_t)(L + LDS_PF) + lw * 256;
  auto pfWs = [&](int st) {
    touch<0>((I.sig >> 1) ? P.wsi : P.wsr,
                            (I.rho * 1024 + I.loc * 32 + (lane >> 1)) * (S * 2) + (I.sig & 1) * 2048 + (st + (lane & 1)) * 128, sink);
  };
  auto pf = [&](int step) {
    if (NB == 4 && lw == 3 && step >= WS_0 && (step - WS_0) * 2 < PF_WSN) pfWs((step - WS_0) * 2);
  };
  auto arm = [&](int i) {
    const u32x4 pz = {POISON, POISON, POISON, POISON};
    __builtin_amdgcn_raw_buffer_store_b128(pz, rws, WS_XP + I.wg * 27648 + min(lw * 7 + i, 26) * 1024 + vl, 0, AUX_SC1);
  };
  STAMP(0); RSTAMP(13);
  if (lw >= 2) dma(mk(P.gw, D * 2), L + LDS_GW + (lw - 2) * 1024, vl + (lw - 2) * 1024, 0);
  int bi = 0; (void)bi;
  issueA(0); issueA(1); issueA(2);
  asm volatile("" ::"s"(tag));
  wait_vm<2 * N1>(); LBAR(); issueA(3);
  STAMP(1);
#pragma unroll 1
  for (int k = 0; k < 5; ++k) { wait_vm<2 * N1>(); LBAR(); issueA(k + 4); }
  wait_vm<2 * N1>(); LBAR(); issueBw(0);
  wait_vm<N1 + NB>(); LBAR(); issueBx(0);
  wait_vm<N2>(); LBAR(); issueBw(1);
  LBAR(); issueBx(1);
  STAMP(2);
  wait_vm<N2>(); LBAR(); issueB(2); pf(10);
  const AS1 unsigned* pfa = wsu + WS_FA / 4 + I.cb * 64 + I.sig * 16 + (lane & 15);
  const AS1 unsigned long long* pg = (const AS1 unsigned long long*)(wsu + WS_G / 4) + I.rb256 * 256 + lw * 64 + lane;
  unsigned f = 0;
  u32x2 gg = {0u, 0u};
#pragma unroll 1
  for (int st = 3; st < 9; ++st) {
    wait_vm<N2>(); LBAR();
    if (st == 5 && lw == 0 && lane < 2)
      flag_store(wsu + (lane ? WS_FW / 4 + I.wg : WS_FA / 4 + (I.pc & 3) * 64 + (I.pc >> 2) * 32 + I.rb64), tag);
    if (st == 8) {
      f = poll_issue(pfa);
      asm volatile("global_load_dwordx2 %0, %1, off sc1" : "=v"(gg) : "v"(pg) : "memory");
    }
    arm(st - 3);
    issueB(st);
    pf(st + 8);
  }
  wait_vm<N2>(); LBAR();
  poll_check(f, pfa, tag);
  arm(6); issueC(0); pf(17);
  {
    asm volatile("" : "+v"(gg));
    unsigned spins = 0;
#pragma unroll 1
    while (!__all(gg[1] == tag)) {
      __builtin_amdgcn_s_sleep(2);
      asm volatile("global_load_dwordx2 %0, %1, off sc1\n\ts_waitcnt vmcnt(0)" : "=&v"(gg) : "v"(pg) : "memory");
      if (++spins > SPIN_MAX) break;
    }
    asm volatile("ds_write_b32 %0, %1\n\ts_waitcnt lgkmcnt(0)" ::"v"((unsigned)(uintptr_t)(L + LDS_G + (lw * 64 + lane) * 4)), "v"(gg[0]) : "memory");
  }
  wait_vm<N3>(); LBAR(); issueC(1); pf(18);
  LBAR(); issueC(2); issueC(3); pf(19);
  STAMP(3);
  wait_vm<0>(); LBAR();
  const AS1 unsigned* pfw = wsu + WS_FW / 4 + I.sig * 64 + lane;
  unsigned f2 = 0;
  const AS1 unsigned* pfh = wsu + WS_FH / 4 + I.rb256 * 32 + ((lane & 8) ? (lane & 3) * 8 + I.j : I.sig * 8 + (lane & 7));
#pragma unroll 1
  for (int u = 0; u < 12; ++u) {
    wait_vm<2 * N3>(); LBAR();
    if (u == 8 && lw == 0 && lane == 0) flag_store(wsu + WS_FH / 4 + I.rb256 * 32 + I.sig * 8 + I.j, tag);
    if (u == 10) { f = poll_issue(pfh); f2 = poll_issue(pfw); }
    issueC(u + 4);
  }
  wait_vm<2 * N3>(); LBAR();
  STAMP(4);
  poll_check(f, pfh, tag);
  poll_check(f2, pfw, tag);
  STAMP(5);
  issueD(16);
#pragma unroll 1
  for (int u = 13; u < 30; ++u) {
    wait_vm<2 * N3>(); LBAR();
    issueD(u + 4);
  }
  wait_vm<2 * N3>(); LBAR();
  wait_vm<N3>(); LBAR();
  wait_vm<0>(); LBAR();
  LBAR();
  STAMP(6);
  if (I.wg == 0 && lw == 1) {
    const AS1 unsigned* pa = wsu + WS_FA / 4 + lane;
#pragma unroll 1
    for (int i = 0; i < 4; ++i) poll_check(0u - (tag == 0u), pa + i * 64, tag);
    if (lane == 0) flag_store(wsu + WS_EPOCH / 4, tag);
  }
  BAR();
  BAR();
  STAMP(7);
}

template <bool DENSE, bool Z = false>
__device__ __forceinline__ void unit(f32x4 (&acc)[4][9], const bf16x8 (&Ac)[4], bf16x8 (&An)[4], bf16x8 (&B)[7],
                                     const bf16x8 (&Xc)[2], bf16x8 (&Xn)[2], AS3 char* sl, int ao, int d2, int bo, int to) {
  SCHED();
  MFMA_Z(acc[0][0], B[0], Ac[0]);
  SCHED();
  AS3 char* a = sl + ao;
  AS3 char* b = sl + bo;
  AS3 char* a2 = a + d2;
  AS3 char* bt = sl + to;
#pragma unroll
  for (int n = 0; n < 7; ++n) {
    unsigned ad = (unsigned)(uintptr_t)b, ax = ad;
    if (n > 0) MFMA_Z(acc[0][n], B[n], Ac[0]);
    SCHED();
    if (n < 4) An[n] = ld128((n < 2 ? a : a2) + (n & 1) * 2048);
    if (DENSE && n == 0) asm volatile("v_xor_b32 %0, 0xe0, %1" : "=v"(ax) : "v"(b));
    SCHED();
    MFMA_Z(acc[1][n], B[n], Ac[1]);
    SCHED();
    if (n < 2) Xn[n] = (DENSE && n == 0) ? ldtr<4096>((AS3 char*)ax) : ldb<DENSE>(b, bt, 7 + n);
    SCHED();
    MFMA_Z(acc[2][n], B[n], Ac[2]);
    SCHED();
    if (DENSE && n > 0) asm volatile("v_xor_b32 %0, %2, %1" : "=v"(ad) : "v"(b), "n"(n << 5));
    SCHED();
    MFMA_Z(acc[3][n], B[n], Ac[3]);
    SCHED();
    B[n] = DENSE ? ldtr<4096>((AS3 char*)ad) : ldb<false>(b, bt, n);
    SCHED();
  }
#pragma unroll
  for (int n = 0; n < 2; ++n)
#pragma unroll
    for (int m = 0; m < 4; ++m) MFMA_Z(acc[m][7 + n], Xc[n], Ac[m]);
  SCHED();
  WAIT_LGKM0();
}
template <bool DENSE>
__device__ __forceinline__ void first_read(bf16x8 (&A)[4], bf16x8 (&B)[7], bf16x8 (&X)[2], AS3 char* a, AS3 char* a2, AS3 char* b, AS3 char* bt) {
#pragma unroll
  for (int m = 0; m < 4; ++m) A[m] = ld128((m < 2 ? a : a2) + (m & 1) * 2048);
#pragma unroll
  for (int n = 0; n < 7; ++n) B[n] = ldb<DENSE>(b, bt, n);
#pragma unroll
  for (int n = 0; n < 2; ++n) X[n] = ldb<DENSE>(b, bt, 7 + n);
  WAIT_LGKM0();
}
template <int ODD>
__device__ __forceinline__ void store_h(const f32x4 (&acc)[4][9], const float (&om)[4], rsrc_t rws, int sbase, int r, int q) {
  const int vp = ((((q & 1) << 1) | (q >> 1)) * 16 + r) * 16 + sbase + ODD * 1024;
  const int vh = ((ODD * 2 + (q >> 1)) * 16 + r) * 16 + (q & 1) * 8 + sbase + (ODD ? 0 : 4096);
#pragma unroll
  for (int m = 0; m < 4; ++m) {
    f32x4 t[9];
#pragma unroll
    for (int n = 0; n < 9; ++n)
#pragma unroll
      for (int e = 0; e < 4; ++e) t[n][e] = acc[m][n][e] * om[m];
#pragma unroll
    for (int p = 0; p < 4; ++p)
      __builtin_amdgcn_raw_buffer_store_b128(relu4(swap_pair(t[2 * p + ODD], t[2 * p + 1 + ODD])), rws,
                                             vp + m * 147456 + p * 1024, 0, AUX_SC1);
    const f32x4 s = t[ODD ? 0 : 8];
    u32x2 v = {relu2(pk(s[0], s[1])), relu2(pk(s[2], s[3]))};
    __builtin_amdgcn_raw_buffer_store_b64(v, rws, vh + m * 147456, 0, AUX_SC1);
  }
}

__device__ __forceinline__ void consumer(const Params& P, const Ids& I, AS3 char* L, int cw, int lane, unsigned tag) {
  const int wg = I.wg, wave = cw;
  (void)wg; (void)wave;
  const int r = lane & 15, q = lane >> 4;
  const rsrc_t rws = mk(P.ws, WS_END);
  if (PRIO) __builtin_amdgcn_s_setprio(PRIO);
  STAMP(0);
  asm volatile("" ::"s"(tag));
  const unsigned sink = (unsigned)(uintptr_t)(L + LDS_PF) + cw * 256;
  if (PF_X && cw == 0) touch<0>(P.x, (I.wg * 72 + lane) * 128, sink);
  if (PF_X && cw == 1)
    touch<0>(lane < 8 ? P.x : (I.wg < 128 ? P.wdr : P.wdi), 0, sink, lane < 8 ? (I.wg * 72 + 64 + lane) * 128 : min((I.wg & 127) * 41 + lane - 8, 5183) * 128);
  const int rc = min(cw * 64 + lane, 143), rf = I.wg * 18 + (rc >> 3);
  const u32x4 rv = *(const u32x4*)((const char*)P.w2 + rf * (D * 2) + 1024 + (rc & 7) * 16);
  const int aL0 = r * 128 + ((q ^ ((r >> 1) & 7)) << 4), aL1 = aL0 ^ 64;
  const int bL = (4 * q + ((lane >> 2) & 3)) * 288 + (lane & 3) * 8;
  f32x4 acc[4][9];
  {
    f32x4 lg = {0.f, 0.f, 0.f, 0.f};
#pragma unroll
    for (int n = 0; n < 9; ++n) { acc[0][n] = f32x4{0.f, 0.f, 0.f, 0.f}; asm volatile("" : "+v"(acc[0][n])); }
    asm volatile("" : "+v"(lg));
#pragma unroll
    for (int m = 1; m < 4; ++m)
#pragma unroll
      for (int n = 0; n < 9; ++n) { acc[m][n] = f32x4{0.f, 0.f, 0.f, 0.f}; asm volatile("" : "+v"(acc[m][n])); }
    bf16x8 xa, xb, w0[10], w1[10];
    BAR();
    asm volatile("" ::"v"(rv));
    STAMP(1);
    xa = ld128(L + aL0 + cw * 2048); xb = ld128(L + aL1 + cw * 2048);
#pragma unroll
    for (int n = 0; n < 9; ++n) { w0[n] = ld128(L + aL0 + 8192 + n * 2048); w1[n] = ld128(L + aL1 + 8192 + n * 2048); }
    w0[9] = ld128(L + LDS_GW + I.ra * 128 + q * 16); w1[9] = ld128(L + LDS_GW + I.ra * 128 + q * 16 + 64);
    WAIT_LGKM0();
    NOP_VALU_WRITE();
    int hs = HS;
#pragma unroll 1
    for (int st = 0; st < 9; ++st) {
      BAR();
      AS3 char* b0 = L + hs + aL0;
      AS3 char* b1 = L + hs + aL1;
      AS3 char* bg = L + LDS_GW + rot9(st + 1, I.ra) * 128 + q * 16;
      SCHED();
#pragma unroll
      for (int n = 0; n < 9; ++n) { MFMA_ACC(acc[0][n], w0[n], xa); SCHED(); w0[n] = ld128(b0 + 8192 + n * 2048); SCHED(); }
      MFMA_ACC(lg, w0[9], xa); SCHED(); w0[9] = ld128(bg); xa = ld128(b0 + cw * 2048); SCHED();
#pragma unroll
      for (int n = 0; n < 9; ++n) { MFMA_ACC(acc[0][n], w1[n], xb); SCHED(); w1[n] = ld128(b1 + 8192 + n * 2048); SCHED(); }
      MFMA_ACC(lg, w1[9], xb); SCHED(); w1[9] = ld128(bg + 64); xb = ld128(b1 + cw * 2048); SCHED();
      WAIT_LGKM0();
      hs = (hs == 3 * HS) ? 0 : hs + HS;
    }
    NOP_MFMA_READ();
    STAMP(2);
    if (cw < 3) {
      const int k = rf & 31, ro = (rf & ~31) + ((k >> 2) & 1) * 16 + (k >> 3) * 4 + (k & 3);
      __builtin_amdgcn_raw_buffer_store_b128(rv, rws, WS_W2T + (((rc & 7) >> 1) * F + ro) * 32 + (rc & 1) * 16, 0, AUX_SC1);
    }
    SCHED();
    if (q == 0 && (r >> 1) == I.pc) {
      const float g = rbf(1.f / (1.f + __expf(-rbf(lg[0]))));
      __hip_atomic_store((AS1 unsigned long long*)((AS1 char*)P.ws + WS_G) + I.rb64 * 64 + cw * 16 + r,
                         ((unsigned long long)tag << 32) | __float_as_uint(g), __ATOMIC_RELAXED, __HIP_MEMORY_SCOPE_AGENT);
    }
    const float sg = (I.pc >> 2) ? -1.f : 1.f;
    const int sl = cw * 16 + r, k = sl & 31;
    const int rho = (sl & 32) + ((k >> 2) & 1) * 16 + (k >> 3) * 4 + (k & 3);
    const int sb = WS_AB + (((I.pc >> 2) * 32 + I.rb64) * 4 + (I.pc & 3)) * BIMG;
    const int vp = rho * 256 + ((q & 1) ^ (rho & 7)) * 32 + (q >> 1) * 16;
#pragma unroll
    for (int p = 0; p < 4; ++p)
      __builtin_amdgcn_raw_buffer_store_b128(swap_pair(acc[0][2 * p] * sg, acc[0][2 * p + 1] * sg), rws, sb + (vp ^ (p * 64)), 0, AUX_SC1);
    const f32x4 s = acc[0][8] * sg;
    u32x2 v = {pk(s[0], s[1]), pk(s[2], s[3])};
    __builtin_amdgcn_raw_buffer_store_b64(v, rws, sb + 16384 + rho * 32 + q * 8, 0, AUX_SC1);
  }
  const int mh = cw >> 1, kg = cw & 1;
  bf16x8 A0[4], A1[4], B[7], X0[2], X1[2];
  {
#pragma unroll
    for (int n = 0; n < 9; ++n) { acc[0][n] = f32x4{0.f, 0.f, 0.f, 0.f}; asm volatile("" : "+v"(acc[0][n])); }
    const int a0 = aL0 + cw * 8192, a1 = aL1 + cw * 8192, b0 = bL + 32768;
    BAR();
    STAMP(3);
    int sl = 2 * BS;
    first_read<false>(A0, B, X0, L + sl + a0, L + sl + a0 + 4096, L + sl + b0, L);
    NOP_VALU_WRITE();
#pragma unroll 1
    for (int st = 0; st < 9; ++st) {
      if (st == 2) wait_vm<0>();
      unit<false>(acc, A0, A1, B, X0, X1, L + sl, a1, 4096, b0 + 9216, 0);
      BAR();
      sl = (sl == 0) ? 2 * BS : sl - BS;
      unit<false>(acc, A1, A0, B, X1, X0, L + sl, a0, 4096, b0, 0);
    }
    NOP_MFMA_READ();
    STAMP(4);
    float om[4];
#pragma unroll
    for (int m = 0; m < 4; ++m) {
      om[m] = rbf(1.f - *(AS3 float*)(L + LDS_G + (cw * 64 + m * 16 + r) * 4));
    }
    const int sb = WS_H + ((I.rb256 * 16 + cw * 4) * 144 + I.sig * 36 + ((I.j * 9) >> 1)) * 1024;
    if (I.j & 1) store_h<1>(acc, om, rws, sb, r, q);
    else store_h<0>(acc, om, rws, sb, r, q);
    STAMP(5);
  }
  {
    asm volatile("" : "+v"(lane));
    const int aS = (kg ? aL1 : aL0) + mh * 8192 + kg * 4096, aH = mh * 8192 + kg * 5120 + lane * 16;
    const int d2 = kg ? -4096 : 4096;
    const int br = kg * 32 + (lane >> 2 & 15);
    const int bb = 16384 + br * 256 + (br & 7) * 32 + (lane & 3) * 8, bt = 32768 + br * 32 + (lane & 3) * 8;
    BAR();
    STAMP(6);
    first_read<true>(A0, B, X0, L + cslot(0) + aS, L + cslot(0) + aS + d2, L + cslot(0) + bb, L + cslot(0) + bt);
    BAR();
    unit<true, true>(acc, A0, A1, B, X0, X1, L + cslot(1), aS, d2, bb, bt);
    BAR();
    unit<true>(acc, A1, A0, B, X1, X0, L + cslot(2), aS, d2, bb, bt);
#pragma unroll 1
    for (int u = 2; u < 16; u += 2) {
      if (u == 8) wait_vm<0>();
      const int s1 = cslot(u + 1), s2 = cslot(u + 2), an = u < 14 ? aS : aH;
      asm volatile("" ::"s"(s1), "s"(s2), "v"(an));
      BAR();
      unit<true>(acc, A0, A1, B, X0, X1, L + s1, aS, d2, bb, bt);
      BAR();
      unit<true>(acc, A1, A0, B, X1, X0, L + s2, an, d2, bb, bt);
    }
    NOP_MFMA_READ();
    STAMP(7);
#pragma unroll
    for (int m = 0; m < 4; ++m) {
      const float g = *(AS3 float*)(L + LDS_G + ((I.j & 1) * 128 + mh * 64 + (m ^ (2 * kg)) * 16 + (lane & 15)) * 4);
#pragma unroll
      for (int n = 0; n < 9; ++n) { acc[m][n] *= g; asm volatile("" : "+v"(acc[m][n])); }
    }
    NOP_VALU_WRITE();
#pragma unroll 1
    for (int u = 16; u < 34; u += 2) {
      const int s1 = cslot(u + 1), s2 = cslot(u + 2);
      asm volatile("" ::"s"(s1), "s"(s2));
      BAR();
      unit<true>(acc, A0, A1, B, X0, X1, L + s1, aH, d2, bb, bt);
      BAR();
      unit<true>(acc, A1, A0, B, X1, X0, L + s2, aH, d2, bb, bt);
    }
    NOP_MFMA_READ();
    STAMP(8);
  }
  {
    asm volatile("" : "+v"(lane));
    AS3 char* wr = L + cw * BIMG + lane * 16;
    AS3 char* rd = L + (cw ^ 1) * BIMG + lane * 16;
#pragma unroll
    for (int m = 0; m < 2; ++m)
#pragma unroll
      for (int n = 0; n < 9; ++n) *(AS3 f32x4*)(wr + (m * 9 + n) * 1024) = acc[2 + m][n];
    WAIT_LGKM0();
    BAR();
    STAMP(10);
#pragma unroll
    for (int m = 0; m < 2; ++m)
#pragma unroll
      for (int n = 0; n < 9; ++n) acc[m][n] += *(AS3 f32x4*)(rd + (m * 9 + n) * 1024);
    if (cw == I.sig) {
      AS3 char* kp = L + 4 * BIMG + lane * 16;
#pragma unroll
      for (int m = 0; m < 2; ++m) {
#pragma unroll
        for (int n = 0; n < 8; ++n) *(AS3 f32x4*)(kp + (m * 8 + n) * 1024) = acc[m][n];
        *(AS3 f32x4*)(kp + (16 + m) * 1024) = acc[m][8];
      }
      WAIT_LGKM0();
    } else {
      const int sb = WS_XP + ((I.loc * 8 + I.rho * 4 + cw) * 3 + ((I.sig - cw - 1) & 3)) * 9216 + lane * 16;
#pragma unroll
      for (int it = 0; it < 9; ++it) {
        const f32x4 a = it < 8 ? acc[it >> 2][(it & 3) * 2] : acc[0][8];
        const f32x4 b = it < 8 ? acc[it >> 2][(it & 3) * 2 + 1] : acc[1][8];
        u32x4 v = {min(pk(a[0], a[1]), POISON - 1), min(pk(a[2], a[3]), POISON - 1), min(pk(b[0], b[1]), POISON - 1), min(pk(b[2], b[3]), POISON - 1)};
        __builtin_amdgcn_raw_buffer_store_b128(v, rws, sb + it * 1024, 0, AUX_SC1);
      }
    }
    STAMP(9);
    BAR();
  }
}

__device__ __forceinline__ void final_items(const Params& P, const Ids& I, AS3 char* L, int w, int lane) {
  const rsrc_t rws = mk(P.ws, WS_END);
  const rsrc_t ro = mk(P.out, S * D * 2);
  const int r = lane & 15, q = lane >> 4;
  const int vb = WS_XP + I.wg * 27648 + lane * 16;
  f32x4 a = *(AS3 f32x4*)(L + 4 * BIMG + w * 2048 + lane * 16);
  f32x4 b = *(AS3 f32x4*)(L + 4 * BIMG + w * 2048 + 1024 + lane * 16);
  f32x4 c = *(AS3 f32x4*)(L + 4 * BIMG + 16384 + (w & 1) * 1024 + lane * 16);
  u32x4 p[3];
  u32x2 e[3];
#pragma unroll
  for (int i = 0; i < 3; ++i) { e[i] = u32x2{0u, 0u}; p[i] = u32x4{0u, 0u, 0u, 0u}; }
  unsigned todo = 0x7;
#pragma unroll 1
  for (unsigned spins = 0; todo && spins < SPIN_MAX; ++spins) {
#pragma unroll
    for (int i = 0; i < 3; ++i)
      if (todo >> i & 1) {
        p[i] = __builtin_amdgcn_raw_buffer_load_b128(rws, vb + i * 9216 + w * 1024, 0, AUX_SC1);
        if (w < 2) e[i] = __builtin_amdgcn_raw_buffer_load_b64(rws, vb + i * 9216 + 8192 + w * 8, 0, AUX_SC1);
      }
#pragma unroll
    for (int i = 0; i < 3; ++i)
      if (todo >> i & 1) {
        const unsigned mx = max(max(max(p[i][0], p[i][1]), max(p[i][2], p[i][3])), max(e[i][0], e[i][1]));
        if (__all(mx != POISON)) todo &= ~(1u << i);
      }
    if (todo) __builtin_amdgcn_s_sleep(POLL_SLEEP);
  }
#pragma unroll
  for (int i = 0; i < 3; ++i) {
    a += f32x4{blo(p[i][0]), bhi(p[i][0]), blo(p[i][1]), bhi(p[i][1])};
    b += f32x4{blo(p[i][2]), bhi(p[i][2]), blo(p[i][3]), bhi(p[i][3])};
    c += f32x4{blo(e[i][0]), bhi(e[i][0]), blo(e[i][1]), bhi(e[i][1])};
  }
  const int row0 = I.rb128 * 128 + I.sig * 32, col0 = I.cb * 128;
  __builtin_amdgcn_raw_buffer_store_b128(swap_pair(a, b), ro, (row0 + (w >> 2) * 16 + r) * (D * 2) + (col0 + (w & 3) * 32) * 2 + (((q & 1) << 1) | (q >> 1)) * 16, 0, 0);
  if (w < 2) {
    u32x2 v = {pk(c[0], c[1]), pk(c[2], c[3])};
    __builtin_amdgcn_raw_buffer_store_b64(v, ro, (row0 + w * 16 + r) * (D * 2) + (512 + I.cb * 16) * 2 + q * 8, 0, 0);
  }
}
}

__global__ __launch_bounds__(512, 2) void gated_mix_fnet_ffn_kernel(Params P) {
  __shared__ __attribute__((aligned(1024))) char lds[LDS_BYTES];
  AS3 char* L = (AS3 char*)lds;
  const int lane = threadIdx.x & 63;
  const int wave = __builtin_amdgcn_readfirstlane(threadIdx.x >> 6);
  Ids I;
  asm volatile("s_getpc_b64 %0" : "=s"(I.pc0));
  I.wg = blockIdx.x;
  if (wave == 0)
    touch<0>((const void*)I.pc0, min(((I.wg >> 3) * PF_CL + (lane & (PF_CL - 1))) * 128, CODE_PF_BYTES), (unsigned)(uintptr_t)(L + LDS_PF));
  I.rho = (I.wg >> 2) & 1; I.sig = I.wg & 3; I.loc = I.wg >> 3;
  I.rb64 = 16 * I.rho + (I.loc >> 1); I.pc = 2 * I.sig + (I.loc & 1);
  I.rb256 = 4 * I.rho + (I.loc >> 3); I.j = I.loc & 7;
  I.rb128 = 2 * I.rb256 + (I.j & 1); I.cb = I.j >> 1;
  I.ra = (I.rb64 & 15) % 9;
  const unsigned tag = *(const __attribute__((address_space(4))) unsigned*)((uintptr_t)P.ws + WS_EPOCH) + 1u;
  if (wave < 4) consumer(P, I, L, wave, lane, tag);
  else if (wave < 6) loader<5>(P, I, L, wave - 4, lane, tag);
  else loader<4>(P, I, L, wave - 4, lane, tag);
  {
    int tz = threadIdx.x;
    asm volatile("" : "+v"(tz));
    const int w2 = __builtin_amdgcn_readfirstlane(tz >> 6);
    final_items(P, I, L, w2, tz & 63);
  }
}

extern "C" void kernel_launch(void* const* d_in, const int* in_sizes, int n_in, void* d_out, int out_size, void* d_ws,
                              size_t ws_size, hipStream_t stream) {
  Params P;
  P.x = d_in[0]; P.wsr = d_in[1]; P.wsi = d_in[2]; P.wdr = d_in[3]; P.wdi = d_in[4];
  P.w1 = d_in[6]; P.w2 = d_in[7]; P.gw = d_in[8]; P.out = d_out; P.ws = d_ws;
  gated_mix_fnet_ffn_kernel<<<256, 512, 0, stream>>>(P);
}
```

```cpp
#include <hip/hip_runtime.h>
#include <hip/hip_bf16.h>
#include <stdint.h>

typedef __attribute__((ext_vector_type(8))) short bf16x8;
typedef __attribute__((ext_vector_type(4))) short s16x4;
typedef __attribute__((ext_vector_type(4))) float f32x4;
typedef __attribute__((ext_vector_type(2))) float f32x2;
typedef __attribute__((ext_vector_type(4))) unsigned u32x4;
typedef __attribute__((ext_vector_type(2))) unsigned u32x2;
typedef __attribute__((ext_vector_type(2))) __bf16 bf16x2_t;
#define AS3 __attribute__((address_space(3)))
#define AS1 __attribute__((address_space(1)))
typedef __amdgpu_buffer_rsrc_t rsrc_t;

namespace {
constexpr int S = 2048, D = 576, F = 4608;
constexpr int WS_EPOCH = 0;
constexpr int WS_FA = 4096;
constexpr int WS_FH = 8192;
constexpr int WS_FW = 12288;
constexpr int WS_G = 32768;
constexpr int WS_PROF = 64 << 20;
constexpr int WS_W2T = 1 << 17;
constexpr int WS_AB = 1 << 20;
constexpr int WS_XP = 6 << 20;
constexpr int WS_H = 16 << 20;
constexpr int WS_END = WS_H + 128 * 144 * 1024;
constexpr int HS = 26624;
constexpr int BS = 53248;
constexpr int LDS_PF = 3 * BS;
constexpr int LDS_GW = LDS_PF + 1024;
constexpr int LDS_G = LDS_GW + 2048;
constexpr int LDS_BYTES = LDS_G + 1024;
constexpr int BIMG = 18432;
constexpr int CODE_PF_BYTES = 30208;
constexpr unsigned SPIN_MAX = 1u << 18;
constexpr int AUX_SC1 = 16;
constexpr unsigned POISON = 0xffffffffu;

constexpr int POLL_SLEEP = 2, PF_X = 1, PRIO = 1, AUX_AB = 16, AUX_H = 16, AUX_WS = 0, AUX_W1 = 0, AUX_W2 = 0, AUX_WD = 0, WS_0 = 12, PF_WSN = 8, PF_CL = 8;

struct Params {
  const void* x; const void* wsr; const void* wsi; const void* wdr; const void* wdi;
  const void* w1; const void* w2; const void* gw; void* out; void* ws;
};

__device__ __forceinline__ rsrc_t mk(const void* p, int bytes) {
  return __builtin_amdgcn_make_buffer_rsrc((void*)p, (short)0, bytes, 0x00020000);
}
__device__ __forceinline__ int cslot(int s) {
  int i = s & 3;
  return i == 0 ? 124928 : (i == 1 ? 69632 : (i == 2 ? 0 : 34816));
}
__device__ __forceinline__ unsigned pk(float a, float b) {
  f32x2 v = {a, b};
  return __builtin_bit_cast(unsigned, __builtin_convertvector(v, bf16x2_t));
}
__device__ __forceinline__ float blo(unsigned u) { return __uint_as_float(u << 16); }
__device__ __forceinline__ float bhi(unsigned u) { return __uint_as_float(u & 0xffff0000u); }
__device__ __forceinline__ float rbf(float a) { return blo(pk(a, 0.f)); }
__device__ __forceinline__ bf16x8 ld128(AS3 char* p) { return *(AS3 bf16x8*)p; }
template <int HI = 4608>
__device__ __forceinline__ bf16x8 ldtr(AS3 char* p) {
  s16x4 lo = __builtin_amdgcn_ds_read_tr16_b64_v4i16((AS3 s16x4*)p);
  s16x4 hi = __builtin_amdgcn_ds_read_tr16_b64_v4i16((AS3 s16x4*)(p + HI));
  return __builtin_shufflevector(lo, hi, 0, 1, 2, 3, 4, 5, 6, 7);
}
template <bool DENSE>
__device__ __forceinline__ bf16x8 ldb(AS3 char* b, AS3 char* bt, int n) {
  if (!DENSE) return ldtr<4608>(b + n * 32);
  if (n == 8) return ldtr<512>(bt);
  return ldtr<4096>((AS3 char*)((unsigned)(uintptr_t)b ^ (unsigned)(n << 5)));
}
#define MFMA(a, b, c) __builtin_amdgcn_mfma_f32_16x16x32_bf16(a, b, c, 0, 0, 0)
#define MFMA_ACC(c, a, b) asm volatile("v_mfma_f32_16x16x32_bf16 %0, %1, %2, %0" : "+v"(c) : "v"(a), "v"(b))
#define MFMA_NEW(c, a, b) asm volatile("v_mfma_f32_16x16x32_bf16 %0, %1, %2, 0" : "=&v"(c) : "v"(a), "v"(b))
#define MFMA_Z(c, a, b) do { if (Z) MFMA_NEW(c, a, b); else MFMA_ACC(c, a, b); } while (0)
#define NOP_MFMA_READ() do { SCHED(); asm volatile("s_nop 7\n\ts_nop 3"); SCHED(); } while (0)
#define NOP_VALU_WRITE() do { SCHED(); asm volatile("s_nop 3"); SCHED(); } while (0)
#define BAR() do { asm volatile("" ::: "memory"); __builtin_amdgcn_s_barrier(); asm volatile("" ::: "memory"); } while (0)
#define SCHED() __builtin_amdgcn_sched_barrier(0)
#define WAIT_LGKM0() __builtin_amdgcn_s_waitcnt(0xC07F)
template <int N> __device__ __forceinline__ void wait_vm() {
  asm volatile("s_waitcnt vmcnt(%0)" ::"n"(N) : "memory");
}
template <int AUX = 0>
__device__ __forceinline__ void dma(rsrc_t r, AS3 char* l, int voff, int soff) {
  __builtin_amdgcn_raw_ptr_buffer_load_lds(r, (AS3 void*)l, 16, voff, soff, 0, AUX);
}
template <int NT>
__device__ __forceinline__ void touch(const void* base, int off, unsigned lds_sink) {
  unsigned keep;
  if (NT)
    asm volatile("s_mov_b32 %0, m0\n\ts_mov_b32 m0, %3\n\ts_nop 0\n\tglobal_load_lds_dword %1, %2 nt\n\ts_mov_b32 m0, %0"
                 : "=&s"(keep) : "v"(off), "s"(base), "s"(lds_sink) : "memory");
  else
    asm volatile("s_mov_b32 %0, m0\n\ts_mov_b32 m0, %3\n\ts_nop 0\n\tglobal_load_lds_dword %1, %2\n\ts_mov_b32 m0, %0"
                 : "=&s"(keep) : "v"(off), "s"(base), "s"(lds_sink) : "memory");
}
template <int NT>
__device__ __forceinline__ void touch(const void* base, int, unsigned lds_sink, int off) {
  unsigned keep;
  asm volatile("s_mov_b32 %0, m0\n\ts_mov_b32 m0, %2\n\ts_nop 0\n\tglobal_load_lds_dword %1, off\n\ts_mov_b32 m0, %0"
               : "=&s"(keep) : "v"((const char*)base + off), "s"(lds_sink) : "memory");
}
__device__ __forceinline__ u32x4 swap_pair(const f32x4& a, const f32x4& b) {
  unsigned x0 = pk(a[0], a[1]), x1 = pk(a[2], a[3]), y0 = pk(b[0], b[1]), y1 = pk(b[2], b[3]);
  auto s0 = __builtin_amdgcn_permlane16_swap(x0, y0, false, false);
  auto s1 = __builtin_amdgcn_permlane16_swap(x1, y1, false, false);
  u32x4 v = {s0[0], s1[0], s0[1], s1[1]};
  return v;
}
__device__ __forceinline__ unsigned relu2(unsigned v) {
  typedef __attribute__((ext_vector_type(2))) short s16x2;
  return __builtin_bit_cast(unsigned, __builtin_elementwise_max(__builtin_bit_cast(s16x2, v), s16x2{0, 0}));
}
__device__ __forceinline__ u32x4 relu4(u32x4 v) { return u32x4{relu2(v[0]), relu2(v[1]), relu2(v[2]), relu2(v[3])}; }
__device__ __forceinline__ unsigned poll_issue(const AS1 unsigned* p) {
  unsigned f;
  asm volatile("global_load_dword %0, %1, off sc1" : "=v"(f) : "v"(p) : "memory");
  return f;
}
__device__ __forceinline__ void poll_check(unsigned f, const AS1 unsigned* p, unsigned tag) {
  asm volatile("" : "+v"(f));
  unsigned spins = 0;
#pragma unroll 1
  while (!__all(f == tag)) {
    __builtin_amdgcn_s_sleep(2);
    asm volatile("global_load_dword %0, %1, off sc1\n\ts_waitcnt vmcnt(0)" : "=&v"(f) : "v"(p) : "memory");
    if (++spins > SPIN_MAX) break;
  }
}
__device__ __forceinline__ void flag_store(AS1 unsigned* p, unsigned v) {
  __hip_atomic_store(p, v, __ATOMIC_RELAXED, __HIP_MEMORY_SCOPE_AGENT);
}

#define RSTAMP(i) do {} while (0)
#define STAMP(i) do {} while (0)

__device__ __forceinline__ int rot9(int st, int k) { const int t = st + k; return t >= 9 ? t - 9 : t; }
__device__ __forceinline__ int rot4(int st, int k, int lim) { return st < lim ? (st & ~3) | ((st + k) & 3) : st; }
__device__ __forceinline__ int rot8(int st, int k) { return st < 16 ? (st & 8) | ((st + k) & 7) : st; }
struct Ids { uint64_t pc0; int wg, rho, sig, loc, rb64, pc, rb256, j, rb128, cb, ra; };

#define LBAR() BAR()
template <int NB>
__device__ __forceinline__ void loader(const Params& P, const Ids& I, AS3 char* L, int lw, int lane, unsigned tag) {
  const int wg = I.wg, wave = lw + 4;
  (void)wg; (void)wave;
  constexpr int N1 = 2 + NB, N2 = 8 + NB, N3 = 4 + NB;
  const rsrc_t rx = mk(P.x, S * D * 2);
  const rsrc_t rwd = mk((I.pc >> 2) ? P.wdi : P.wdr, D * D * 2);
  const rsrc_t rw1 = mk(P.w1, D * F * 2);
  const rsrc_t rw2 = mk(P.w2, F * D * 2);
  const rsrc_t rs = mk((I.sig >> 1) ? P.wsi : P.wsr, S * S * 2);
  const rsrc_t rws = mk(P.ws, WS_END);
  const int sw = ((lane & 7) ^ (((lw & 1) << 2) + (lane >> 4))) << 4;
  const int vx = (lw * 8 + (lane >> 3)) * (D * 2) + sw;
  const int vs = (lw * 8 + (lane >> 3)) * (S * 2) + sw;
  const int vl = lane * 16;
  const int v2 = (lw * 8 + (lane >> 4)) * (D * 2) + (((lane >> 1 & 7) ^ ((lw * 4 + (lane >> 4)) & 7)) * 2 + (lane & 1)) * 16;
  int v1[NB];
#pragma unroll
  for (int i = 0; i < NB; ++i) {
    int c = (lw + 4 * i) * 64 + lane, r = c / 18, col = c - r * 18;
    int k = (r & 32) + ((r >> 2) & 3) * 8 + ((r >> 4) & 1) * 4 + (r & 3);
    v1[i] = k * (F * 2) + col * 16;
  }
  auto issueAx = [&](int st) {
    AS3 char* b = L + (st & 3) * HS + lw * 1024;
#pragma unroll
    for (int i = 0; i < 2; ++i) dma(rx, b + i * 4096, vx, (I.rb64 * 64 + i * 32) * (D * 2) + rot9(st, I.ra) * 128);
  };
  auto issueAw = [&](int st) {
    AS3 char* b = L + (st & 3) * HS + lw * 1024;
#pragma unroll
    for (int i = 0; i < NB; ++i) dma<AUX_WD>(rwd, b + 8192 + i * 4096, vx, (i < 4 ? (I.pc & 3) * 128 + i * 32 : 512 + (I.pc & 3) * 16) * (D * 2) + rot9(st, I.ra) * 128);
  };
  auto issueA = [&](int st) { issueAx(st); issueAw(st); };
  auto issueBw = [&](int st) {
    AS3 char* b = L + (2 - st % 3) * BS + lw * 1024;
#pragma unroll
    for (int i = 0; i < NB; ++i) dma<AUX_W1>(rw1, b + 32768 + i * 4096, v1[i], rot4(st, I.rb256, 8) * 64 * (F * 2) + (I.sig * 8 + I.j) * 288);
  };
  auto issueBx = [&](int st) {
    AS3 char* b = L + (2 - st % 3) * BS + lw * 1024;
#pragma unroll
    for (int i = 0; i < 8; ++i) dma(rx, b + i * 4096, vx, (I.rb256 * 256 + i * 32) * (D * 2) + rot4(st, I.rb256, 8) * 128);
  };
  auto issueB = [&](int st) { issueBw(st); issueBx(st); };
  auto issueC = [&](int st) {
    AS3 char* b = L + cslot(st) + lw * 1024;
#pragma unroll
    for (int i = 0; i < 4; ++i) {
      const int so = (I.rb128 * 128 + i * 32) * (S * 2) + (I.sig & 1) * 2048 + rot4(st, I.cb, 16) * 128;
      dma<AUX_WS>(rs, b + i * 4096, vs, so);
    }
#pragma unroll
    for (int i = 0; i < NB; ++i) dma<AUX_AB>(rws, b + 16384 + i * 4096, vl, WS_AB + ((I.sig * 16 + rot4(st, I.cb, 16)) * 4 + I.cb) * BIMG + (lw + 4 * i) * 1024);
  };
  auto issueD = [&](int st) {
    AS3 char* b = L + cslot(st) + lw * 1024;
    const int t = rot8(st - 16, I.rb128);
#pragma unroll
    for (int i = 0; i < 4; ++i)
      dma<AUX_W2>(rw2, b + 16384 + i * 4096, v2, (I.sig * 1152 + t * 64 + (i >> 1) * 32 + (i & 1) * 4) * (D * 2) + I.cb * 256);
    if (NB == 5) dma<AUX_SC1>(rws, b + 32768, vl, WS_W2T + (I.cb * F + I.sig * 1152 + t * 64) * 32 + lw * 1024);
#pragma unroll
    for (int i = 0; i < 4; ++i)
      dma<AUX_H>(rws, b + i * 4096, vl, WS_H + ((I.rb128 * 8 + 2 * i + (lw >> 1)) * 144 + I.sig * 36 + 2 * t + (lw & 1)) * 1024);
  };
  AS1 unsigned* wsu = (AS1 unsigned*)P.ws;

  const unsigned sink = (unsigned)(uintptr_t)(L + LDS_PF) + lw * 256;
  auto pfWs = [&](int st) {
    touch<0>((I.sig >> 1) ? P.wsi : P.wsr,
                            (I.rho * 1024 + I.loc * 32 + (lane >> 1)) * (S * 2) + (I.sig & 1) * 2048 + (st + (lane & 1)) * 128, sink);
  };
  auto pf = [&](int step) {
    if (NB == 4 && lw == 3 && step >= WS_0 && (step - WS_0) * 2 < PF_WSN) pfWs((step - WS_0) * 2);
  };
  auto arm = [&](int i) {
    const u32x4 pz = {POISON, POISON, POISON, POISON};
    __builtin_amdgcn_raw_buffer_store_b128(pz, rws, WS_XP + I.wg * 27648 + min(lw * 7 + i, 26) * 1024 + vl, 0, AUX_SC1);
  };
  STAMP(0); RSTAMP(13);
  if (lw >= 2) dma(mk(P.gw, D * 2), L + LDS_GW + (lw - 2) * 1024, vl + (lw - 2) * 1024, 0);
  int bi = 0; (void)bi;
  issueA(0); issueA(1); issueA(2);
  asm volatile("" ::"s"(tag));
  wait_vm<2 * N1>(); LBAR(); issueA(3);
  STAMP(1);
#pragma unroll 1
  for (int k = 0; k < 5; ++k) { wait_vm<2 * N1>(); LBAR(); issueA(k + 4); }
  wait_vm<2 * N1>(); LBAR(); issueBw(0);
  wait_vm<N1 + NB>(); LBAR(); issueBx(0);
  wait_vm<N2>(); LBAR(); issueBw(1);
  LBAR(); issueBx(1);
  STAMP(2);
  wait_vm<N2>(); LBAR(); issueB(2); pf(10);
  const AS1 unsigned* pfa = wsu + WS_FA / 4 + I.cb * 64 + I.sig * 16 + (lane & 15);
  const AS1 unsigned long long* pg = (const AS1 unsigned long long*)(wsu + WS_G / 4) + I.rb256 * 256 + lw * 64 + lane;
  unsigned f = 0;
  u32x2 gg = {0u, 0u};
#pragma unroll 1
  for (int st = 3; st < 9; ++st) {
    wait_vm<N2>(); LBAR();
    if (st == 5 && lw == 0 && lane < 2)
      flag_store(wsu + (lane ? WS_FW / 4 + I.wg : WS_FA / 4 + (I.pc & 3) * 64 + (I.pc >> 2) * 32 + I.rb64), tag);
    if (st == 8) {
      f = poll_issue(pfa);
      asm volatile("global_load_dwordx2 %0, %1, off sc1" : "=v"(gg) : "v"(pg) : "memory");
    }
    arm(st - 3);
    issueB(st);
    pf(st + 8);
  }
  wait_vm<N2>(); LBAR();
  poll_check(f, pfa, tag);
  arm(6); issueC(0); pf(17);
  {
    asm volatile("" : "+v"(gg));
    unsigned spins = 0;
#pragma unroll 1
    while (!__all(gg[1] == tag)) {
      __builtin_amdgcn_s_sleep(2);
      asm volatile("global_load_dwordx2 %0, %1, off sc1\n\ts_waitcnt vmcnt(0)" : "=&v"(gg) : "v"(pg) : "memory");
      if (++spins > SPIN_MAX) break;
    }
    asm volatile("ds_write_b32 %0, %1\n\ts_waitcnt lgkmcnt(0)" ::"v"((unsigned)(uintptr_t)(L + LDS_G + (lw * 64 + lane) * 4)), "v"(gg[0]) : "memory");
  }
  wait_vm<N3>(); LBAR(); issueC(1); pf(18);
  LBAR(); issueC(2); issueC(3); pf(19);
  STAMP(3);
  wait_vm<0>(); LBAR();
  const AS1 unsigned* pfw = wsu + WS_FW / 4 + I.sig * 64 + lane;
  unsigned f2 = 0;
  const AS1 unsigned* pfh = wsu + WS_FH / 4 + I.rb256 * 32 + ((lane & 8) ? (lane & 3) * 8 + I.j : I.sig * 8 + (lane & 7));
#pragma unroll 1
  for (int u = 0; u < 12; ++u) {
    wait_vm<2 * N3>(); LBAR();
    if (u == 8 && lw == 0 && lane == 0) flag_store(wsu + WS_FH / 4 + I.rb256 * 32 + I.sig * 8 + I.j, tag);
    if (u == 10) { f = poll_issue(pfh); f2 = poll_issue(pfw); }
    issueC(u + 4);
  }
  wait_vm<2 * N3>(); LBAR();
  STAMP(4);
  poll_check(f, pfh, tag);
  poll_check(f2, pfw, tag);
  STAMP(5);
  issueD(16);
#pragma unroll 1
  for (int u = 13; u < 30; ++u) {
    wait_vm<2 * N3>(); LBAR();
    issueD(u + 4);
  }
  wait_vm<2 * N3>(); LBAR();
  wait_vm<N3>(); LBAR();
  wait_vm<0>(); LBAR();
  LBAR();
  STAMP(6);
  if (I.wg == 0 && lw == 1) {
    const AS1 unsigned* pa = wsu + WS_FA / 4 + lane;
#pragma unroll 1
    for (int i = 0; i < 4; ++i) poll_check(0u - (tag == 0u), pa + i * 64, tag);
    if (lane == 0) flag_store(wsu + WS_EPOCH / 4, tag);
  }
  BAR();
  BAR();
  STAMP(7);
}

template <bool DENSE, bool Z = false>
__device__ __forceinline__ void unit(f32x4 (&acc)[4][9], const bf16x8 (&Ac)[4], bf16x8 (&An)[4], bf16x8 (&B)[7],
                                     const bf16x8 (&Xc)[2], bf16x8 (&Xn)[2], AS3 char* sl, int ao, int d2, int bo, int to) {
  SCHED();
  MFMA_Z(acc[0][0], B[0], Ac[0]);
  SCHED();
  AS3 char* a = sl + ao;
  AS3 char* b = sl + bo;
  AS3 char* a2 = a + d2;
  AS3 char* bt = sl + to;
#pragma unroll
  for (int n = 0; n < 7; ++n) {
    unsigned ad = (unsigned)(uintptr_t)b, ax = ad;
    if (n > 0) MFMA_Z(acc[0][n], B[n], Ac[0]);
    SCHED();
    if (n < 4) An[n] = ld128((n < 2 ? a : a2) + (n & 1) * 2048);
    if (DENSE && n == 0) asm volatile("v_xor_b32 %0, 0xe0, %1" : "=v"(ax) : "v"(b));
    SCHED();
    MFMA_Z(acc[1][n], B[n], Ac[1]);
    SCHED();
    if (n < 2) Xn[n] = (DENSE && n == 0) ? ldtr<4096>((AS3 char*)ax) : ldb<DENSE>(b, bt, 7 + n);
    SCHED();
    MFMA_Z(acc[2][n], B[n], Ac[2]);
    SCHED();
    if (DENSE && n > 0) asm volatile("v_xor_b32 %0, %2, %1" : "=v"(ad) : "v"(b), "n"(n << 5));
    SCHED();
    MFMA_Z(acc[3][n], B[n], Ac[3]);
    SCHED();
    B[n] = DENSE ? ldtr<4096>((AS3 char*)ad) : ldb<false>(b, bt, n);
    SCHED();
  }
#pragma unroll
  for (int n = 0; n < 2; ++n)
#pragma unroll
    for (int m = 0; m < 4; ++m) MFMA_Z(acc[m][7 + n], Xc[n], Ac[m]);
  SCHED();
  WAIT_LGKM0();
}
template <bool DENSE>
__device__ __forceinline__ void first_read(bf16x8 (&A)[4], bf16x8 (&B)[7], bf16x8 (&X)[2], AS3 char* a, AS3 char* a2, AS3 char* b, AS3 char* bt) {
#pragma unroll
  for (int m = 0; m < 4; ++m) A[m] = ld128((m < 2 ? a : a2) + (m & 1) * 2048);
#pragma unroll
  for (int n = 0; n < 7; ++n) B[n] = ldb<DENSE>(b, bt, n);
#pragma unroll
  for (int n = 0; n < 2; ++n) X[n] = ldb<DENSE>(b, bt, 7 + n);
  WAIT_LGKM0();
}
template <int ODD>
__device__ __forceinline__ void store_h(const f32x4 (&acc)[4][9], const float (&om)[4], rsrc_t rws, int sbase, int r, int q) {
  const int vp = ((((q & 1) << 1) | (q >> 1)) * 16 + r) * 16 + sbase + ODD * 1024;
  const int vh = ((ODD * 2 + (q >> 1)) * 16 + r) * 16 + (q & 1) * 8 + sbase + (ODD ? 0 : 4096);
#pragma unroll
  for (int m = 0; m < 4; ++m) {
    f32x4 t[9];
#pragma unroll
    for (int n = 0; n < 9; ++n)
#pragma unroll
      for (int e = 0; e < 4; ++e) t[n][e] = acc[m][n][e] * om[m];
#pragma unroll
    for (int p = 0; p < 4; ++p)
      __builtin_amdgcn_raw_buffer_store_b128(relu4(swap_pair(t[2 * p + ODD], t[2 * p + 1 + ODD])), rws,
                                             vp + m * 147456 + p * 1024, 0, AUX_SC1);
    const f32x4 s = t[ODD ? 0 : 8];
    u32x2 v = {relu2(pk(s[0], s[1])), relu2(pk(s[2], s[3]))};
    __builtin_amdgcn_raw_buffer_store_b64(v, rws, vh + m * 147456, 0, AUX_SC1);
  }
}

__device__ __forceinline__ void consumer(const Params& P, const Ids& I, AS3 char* L, int cw, int lane, unsigned tag) {
  const int wg = I.wg, wave = cw;
  (void)wg; (void)wave;
  const int r = lane & 15, q = lane >> 4;
  const rsrc_t rws = mk(P.ws, WS_END);
  if (PRIO) __builtin_amdgcn_s_setprio(PRIO);
  STAMP(0);
  asm volatile("" ::"s"(tag));
  const unsigned sink = (unsigned)(uintptr_t)(L + LDS_PF) + cw * 256;
  if (PF_X && cw == 0) touch<0>(P.x, (I.wg * 72 + lane) * 128, sink);
  if (PF_X && cw == 1)
    touch<0>(lane < 8 ? P.x : (I.wg < 128 ? P.wdr : P.wdi), 0, sink, lane < 8 ? (I.wg * 72 + 64 + lane) * 128 : min((I.wg & 127) * 41 + lane - 8, 5183) * 128);
  if (cw >= 2 && I.loc < 16) {
    const int pg = (lane & 31) * 262144 + I.loc * 128;
    if (cw == 2 && I.loc < 8) touch<0>((I.sig >> 1) ? P.wsi : P.wsr, min(pg, S * S * 2 - 4096), sink);
    else if (cw == 2) touch<0>(P.w1, min(pg, D * F * 2 - 4096), sink);
    else if (I.loc < 8) touch<0>(P.w2, min(pg, D * F * 2 - 4096), sink);
    else touch<0>(P.out, min(pg, S * D * 2 - 4096), sink);
  }
  const int rc = min(cw * 64 + lane, 143), rf = I.wg * 18 + (rc >> 3);
  const u32x4 rv = *(const u32x4*)((const char*)P.w2 + rf * (D * 2) + 1024 + (rc & 7) * 16);
  const int aL0 = r * 128 + ((q ^ ((r >> 1) & 7)) << 4), aL1 = aL0 ^ 64;
  const int bL = (4 * q + ((lane >> 2) & 3)) * 288 + (lane & 3) * 8;
  f32x4 acc[4][9];
  {
    f32x4 lg = {0.f, 0.f, 0.f, 0.f};
#pragma unroll
    for (int n = 0; n < 9; ++n) { acc[0][n] = f32x4{0.f, 0.f, 0.f, 0.f}; asm volatile("" : "+v"(acc[0][n])); }
    asm volatile("" : "+v"(lg));
#pragma unroll
    for (int m = 1; m < 4; ++m)
#pragma unroll
      for (int n = 0; n < 9; ++n) { acc[m][n] = f32x4{0.f, 0.f, 0.f, 0.f}; asm volatile("" : "+v"(acc[m][n])); }
    bf16x8 xa, xb, w0[10], w1[10];
    BAR();
    asm volatile("" ::"v"(rv));
    STAMP(1);
    xa = ld128(L + aL0 + cw * 2048); xb = ld128(L + aL1 + cw * 2048);
#pragma unroll
    for (int n = 0; n < 9; ++n) { w0[n] = ld128(L + aL0 + 8192 + n * 2048); w1[n] = ld128(L + aL1 + 8192 + n * 2048); }
    w0[9] = ld128(L + LDS_GW + I.ra * 128 + q * 16); w1[9] = ld128(L + LDS_GW + I.ra * 128 + q * 16 + 64);
    WAIT_LGKM0();
    NOP_VALU_WRITE();
    int hs = HS;
#pragma unroll 1
    for (int st = 0; st < 9; ++st) {
      BAR();
      AS3 char* b0 = L + hs + aL0;
      AS3 char* b1 = L + hs + aL1;
      AS3 char* bg = L + LDS_GW + rot9(st + 1, I.ra) * 128 + q * 16;
      SCHED();
#pragma unroll
      for (int n = 0; n < 9; ++n) { MFMA_ACC(acc[0][n], w0[n], xa); SCHED(); w0[n] = ld128(b0 + 8192 + n * 2048); SCHED(); }
      MFMA_ACC(lg, w0[9], xa); SCHED(); w0[9] = ld128(bg); xa = ld128(b0 + cw * 2048); SCHED();
#pragma unroll
      for (int n = 0; n < 9; ++n) { MFMA_ACC(acc[0][n], w1[n], xb); SCHED(); w1[n] = ld128(b1 + 8192 + n * 2048); SCHED(); }
      MFMA_ACC(lg, w1[9], xb); SCHED(); w1[9] = ld128(bg + 64); xb = ld128(b1 + cw * 2048); SCHED();
      WAIT_LGKM0();
      hs = (hs == 3 * HS) ? 0 : hs + HS;
    }
    NOP_MFMA_READ();
    STAMP(2);
    if (cw < 3) {
      const int k = rf & 31, ro = (rf & ~31) + ((k >> 2) & 1) * 16 + (k >> 3) * 4 + (k & 3);
      __builtin_amdgcn_raw_buffer_store_b128(rv, rws, WS_W2T + (((rc & 7) >> 1) * F + ro) * 32 + (rc & 1) * 16, 0, AUX_SC1);
    }
    SCHED();
    if (q == 0 && (r >> 1) == I.pc) {
      const float g = rbf(1.f / (1.f + __expf(-rbf(lg[0]))));
      __hip_atomic_store((AS1 unsigned long long*)((AS1 char*)P.ws + WS_G) + I.rb64 * 64 + cw * 16 + r,
                         ((unsigned long long)tag << 32) | __float_as_uint(g), __ATOMIC_RELAXED, __HIP_MEMORY_SCOPE_AGENT);
    }
    const float sg = (I.pc >> 2) ? -1.f : 1.f;
    const int sl = cw * 16 + r, k = sl & 31;
    const int rho = (sl & 32) + ((k >> 2) & 1) * 16 + (k >> 3) * 4 + (k & 3);
    const int sb = WS_AB + (((I.pc >> 2) * 32 + I.rb64) * 4 + (I.pc & 3)) * BIMG;
    const int vp = rho * 256 + ((q & 1) ^ (rho & 7)) * 32 + (q >> 1) * 16;
#pragma unroll
    for (int p = 0; p < 4; ++p)
      __builtin_amdgcn_raw_buffer_store_b128(swap_pair(acc[0][2 * p] * sg, acc[0][2 * p + 1] * sg), rws, sb + (vp ^ (p * 64)), 0, AUX_SC1);
    const f32x4 s = acc[0][8] * sg;
    u32x2 v = {pk(s[0], s[1]), pk(s[2], s[3])};
    __builtin_amdgcn_raw_buffer_store_b64(v, rws, sb + 16384 + rho * 32 + q * 8, 0, AUX_SC1);
  }
  const int mh = cw >> 1, kg = cw & 1;
  bf16x8 A0[4], A1[4], B[7], X0[2], X1[2];
  {
#pragma unroll
    for (int n = 0; n < 9; ++n) { acc[0][n] = f32x4{0.f, 0.f, 0.f, 0.f}; asm volatile("" : "+v"(acc[0][n])); }
    const int a0 = aL0 + cw * 8192, a1 = aL1 + cw * 8192, b0 = bL + 32768;
    BAR();
    STAMP(3);
    int sl = 2 * BS;
    first_read<false>(A0, B, X0, L + sl + a0, L + sl + a0 + 4096, L + sl + b0, L);
    NOP_VALU_WRITE();
#pragma unroll 1
    for (int st = 0; st < 9; ++st) {
      if (st == 2) wait_vm<0>();
      unit<false>(acc, A0, A1, B, X0, X1, L + sl, a1, 4096, b0 + 9216, 0);
      BAR();
      sl = (sl == 0) ? 2 * BS : sl - BS;
      unit<false>(acc, A1, A0, B, X1, X0, L + sl, a0, 4096, b0, 0);
    }
    NOP_MFMA_READ();
    STAMP(4);
    float om[4];
#pragma unroll
    for (int m = 0; m < 4; ++m) {
      om[m] = rbf(1.f - *(AS3 float*)(L + LDS_G + (cw * 64 + m * 16 + r) * 4));
    }
    const int sb = WS_H + ((I.rb256 * 16 + cw * 4) * 144 + I.sig * 36 + ((I.j * 9) >> 1)) * 1024;
    if (I.j & 1) store_h<1>(acc, om, rws, sb, r, q);
    else store_h<0>(acc, om, rws, sb, r, q);
    STAMP(5);
  }
  {
    asm volatile("" : "+v"(lane));
    const int aS = (kg ? aL1 : aL0) + mh * 8192 + kg * 4096, aH = mh * 8192 + kg * 5120 + lane * 16;
    const int d2 = kg ? -4096 : 4096;
    const int br = kg * 32 + (lane >> 2 & 15);
    const int bb = 16384 + br * 256 + (br & 7) * 32 + (lane & 3) * 8, bt = 32768 + br * 32 + (lane & 3) * 8;
    BAR();
    STAMP(6);
    first_read<true>(A0, B, X0, L + cslot(0) + aS, L + cslot(0) + aS + d2, L + cslot(0) + bb, L + cslot(0) + bt);
    BAR();
    unit<true, true>(acc, A0, A1, B, X0, X1, L + cslot(1), aS, d2, bb, bt);
    BAR();
    unit<true>(acc, A1, A0, B, X1, X0, L + cslot(2), aS, d2, bb, bt);
#pragma unroll 1
    for (int u = 2; u < 16; u += 2) {
      if (u == 8) wait_vm<0>();
      const int s1 = cslot(u + 1), s2 = cslot(u + 2), an = u < 14 ? aS : aH;
      asm volatile("" ::"s"(s1), "s"(s2), "v"(an));
      BAR();
      unit<true>(acc, A0, A1, B, X0, X1, L + s1, aS, d2, bb, bt);
      BAR();
      unit<true>(acc, A1, A0, B, X1, X0, L + s2, an, d2, bb, bt);
    }
    NOP_MFMA_READ();
    STAMP(7);
#pragma unroll
    for (int m = 0; m < 4; ++m) {
      const float g = *(AS3 float*)(L + LDS_G + ((I.j & 1) * 128 + mh * 64 + (m ^ (2 * kg)) * 16 + (lane & 15)) * 4);
#pragma unroll
      for (int n = 0; n < 9; ++n) { acc[m][n] *= g; asm volatile("" : "+v"(acc[m][n])); }
    }
    NOP_VALU_WRITE();
#pragma unroll 1
    for (int u = 16; u < 34; u += 2) {
      const int s1 = cslot(u + 1), s2 = cslot(u + 2);
      asm volatile("" ::"s"(s1), "s"(s2));
      BAR();
      unit<true>(acc, A0, A1, B, X0, X1, L + s1, aH, d2, bb, bt);
      BAR();
      unit<true>(acc, A1, A0, B, X1, X0, L + s2, aH, d2, bb, bt);
    }
    NOP_MFMA_READ();
    STAMP(8);
  }
  {
    asm volatile("" : "+v"(lane));
    AS3 char* wr = L + cw * BIMG + lane * 16;
    AS3 char* rd = L + (cw ^ 1) * BIMG + lane * 16;
#pragma unroll
    for (int m = 0; m < 2; ++m)
#pragma unroll
      for (int n = 0; n < 9; ++n) *(AS3 f32x4*)(wr + (m * 9 + n) * 1024) = acc[2 + m][n];
    WAIT_LGKM0();
    BAR();
    STAMP(10);
#pragma unroll
    for (int m = 0; m < 2; ++m)
#pragma unroll
      for (int n = 0; n < 9; ++n) acc[m][n] += *(AS3 f32x4*)(rd + (m * 9 + n) * 1024);
    if (cw == I.sig) {
      AS3 char* kp = L + 4 * BIMG + lane * 16;
#pragma unroll
      for (int m = 0; m < 2; ++m) {
#pragma unroll
        for (int n = 0; n < 8; ++n) *(AS3 f32x4*)(kp + (m * 8 + n) * 1024) = acc[m][n];
        *(AS3 f32x4*)(kp + (16 + m) * 1024) = acc[m][8];
      }
      WAIT_LGKM0();
    } else {
      const int sb = WS_XP + ((I.loc * 8 + I.rho * 4 + cw) * 3 + ((I.sig - cw - 1) & 3)) * 9216 + lane * 16;
#pragma unroll
      for (int it = 0; it < 9; ++it) {
        const f32x4 a = it < 8 ? acc[it >> 2][(it & 3) * 2] : acc[0][8];
        const f32x4 b = it < 8 ? acc[it >> 2][(it & 3) * 2 + 1] : acc[1][8];
        u32x4 v = {min(pk(a[0], a[1]), POISON - 1), min(pk(a[2], a[3]), POISON - 1), min(pk(b[0], b[1]), POISON - 1), min(pk(b[2], b[3]), POISON - 1)};
        __builtin_amdgcn_raw_buffer_store_b128(v, rws, sb + it * 1024, 0, AUX_SC1);
      }
    }
    STAMP(9);
    BAR();
  }
}

__device__ __forceinline__ void final_items(const Params& P, const Ids& I, AS3 char* L, int w, int lane) {
  const rsrc_t rws = mk(P.ws, WS_END);
  const rsrc_t ro = mk(P.out, S * D * 2);
  const int r = lane & 15, q = lane >> 4;
  const int vb = WS_XP + I.wg * 27648 + lane * 16;
  f32x4 a = *(AS3 f32x4*)(L + 4 * BIMG + w * 2048 + lane * 16);
  f32x4 b = *(AS3 f32x4*)(L + 4 * BIMG + w * 2048 + 1024 + lane * 16);
  f32x4 c = *(AS3 f32x4*)(L + 4 * BIMG + 16384 + (w & 1) * 1024 + lane * 16);
  u32x4 p[3];
  u32x2 e[3];
#pragma unroll
  for (int i = 0; i < 3; ++i) { e[i] = u32x2{0u, 0u}; p[i] = u32x4{0u, 0u, 0u, 0u}; }
  unsigned todo = 0x7;
#pragma unroll 1
  for (unsigned spins = 0; todo && spins < SPIN_MAX; ++spins) {
#pragma unroll
    for (int i = 0; i < 3; ++i)
      if (todo >> i & 1) {
        p[i] = __builtin_amdgcn_raw_buffer_load_b128(rws, vb + i * 9216 + w * 1024, 0, AUX_SC1);
        if (w < 2) e[i] = __builtin_amdgcn_raw_buffer_load_b64(rws, vb + i * 9216 + 8192 + w * 8, 0, AUX_SC1);
      }
#pragma unroll
    for (int i = 0; i < 3; ++i)
      if (todo >> i & 1) {
        const unsigned mx = max(max(max(p[i][0], p[i][1]), max(p[i][2], p[i][3])), max(e[i][0], e[i][1]));
        if (__all(mx != POISON)) todo &= ~(1u << i);
      }
    if (todo) __builtin_amdgcn_s_sleep(POLL_SLEEP);
  }
#pragma unroll
  for (int i = 0; i < 3; ++i) {
    a += f32x4{blo(p[i][0]), bhi(p[i][0]), blo(p[i][1]), bhi(p[i][1])};
    b += f32x4{blo(p[i][2]), bhi(p[i][2]), blo(p[i][3]), bhi(p[i][3])};
    c += f32x4{blo(e[i][0]), bhi(e[i][0]), blo(e[i][1]), bhi(e[i][1])};
  }
  const int row0 = I.rb128 * 128 + I.sig * 32, col0 = I.cb * 128;
  __builtin_amdgcn_raw_buffer_store_b128(swap_pair(a, b), ro, (row0 + (w >> 2) * 16 + r) * (D * 2) + (col0 + (w & 3) * 32) * 2 + (((q & 1) << 1) | (q >> 1)) * 16, 0, 0);
  if (w < 2) {
    u32x2 v = {pk(c[0], c[1]), pk(c[2], c[3])};
    __builtin_amdgcn_raw_buffer_store_b64(v, ro, (row0 + w * 16 + r) * (D * 2) + (512 + I.cb * 16) * 2 + q * 8, 0, 0);
  }
}
}

__global__ __launch_bounds__(512, 2) void gated_mix_fnet_ffn_kernel(Params P) {
  __shared__ __attribute__((aligned(1024))) char lds[LDS_BYTES];
  AS3 char* L = (AS3 char*)lds;
  const int lane = threadIdx.x & 63;
  const int wave = __builtin_amdgcn_readfirstlane(threadIdx.x >> 6);
  Ids I;
  asm volatile("s_getpc_b64 %0" : "=s"(I.pc0));
  I.wg = blockIdx.x;
  if (wave == 0)
    touch<0>((const void*)I.pc0, min(((I.wg >> 3) * PF_CL + (lane & (PF_CL - 1))) * 128, CODE_PF_BYTES), (unsigned)(uintptr_t)(L + LDS_PF));
  I.rho = (I.wg >> 2) & 1; I.sig = I.wg & 3; I.loc = I.wg >> 3;
  I.rb64 = 16 * I.rho + (I.loc >> 1); I.pc = 2 * I.sig + (I.loc & 1);
  I.rb256 = 4 * I.rho + (I.loc >> 3); I.j = I.loc & 7;
  I.rb128 = 2 * I.rb256 + (I.j & 1); I.cb = I.j >> 1;
  I.ra = (I.rb64 & 15) % 9;
  const unsigned tag = *(const __attribute__((address_space(4))) unsigned*)((uintptr_t)P.ws + WS_EPOCH) + 1u;
  if (wave < 4) consumer(P, I, L, wave, lane, tag);
  else if (wave < 6) loader<5>(P, I, L, wave - 4, lane, tag);
  else loader<4>(P, I, L, wave - 4, lane, tag);
  {
    int tz = threadIdx.x;
    asm volatile("" : "+v"(tz));
    const int w2 = __builtin_amdgcn_readfirstlane(tz >> 6);
    final_items(P, I, L, w2, tz & 63);
  }
}

extern "C" void kernel_launch(void* const* d_in, const int* in_sizes, int n_in, void* d_out, int out_size, void* d_ws,
                              size_t ws_size, hipStream_t stream) {
  Params P;
  P.x = d_in[0]; P.wsr = d_in[1]; P.wsi = d_in[2]; P.wdr = d_in[3]; P.wdi = d_in[4];
  P.w1 = d_in[6]; P.w2 = d_in[7]; P.gw = d_in[8]; P.out = d_out; P.ws = d_ws;
  gated_mix_fnet_ffn_kernel<<<256, 512, 0, stream>>>(P);
}
```

```cpp
#include <hip/hip_runtime.h>
#include <hip/hip_bf16.h>
#include <stdint.h>

typedef __attribute__((ext_vector_type(8))) short bf16x8;
typedef __attribute__((ext_vector_type(4))) short s16x4;
typedef __attribute__((ext_vector_type(4))) float f32x4;
typedef __attribute__((ext_vector_type(2))) float f32x2;
typedef __attribute__((ext_vector_type(4))) unsigned u32x4;
typedef __attribute__((ext_vector_type(2))) unsigned u32x2;
typedef __attribute__((ext_vector_type(2))) __bf16 bf16x2_t;
#define AS3 __attribute__((address_space(3)))
#define AS1 __attribute__((address_space(1)))
typedef __amdgpu_buffer_rsrc_t rsrc_t;

namespace {
constexpr int S = 2048, D = 576, F = 4608;
constexpr int WS_EPOCH = 0;
constexpr int WS_FA = 4096;
constexpr int WS_FH = 8192;
constexpr int WS_FW = 12288;
constexpr int WS_G = 32768;
constexpr int WS_PROF = 64 << 20;
constexpr int WS_W2T = 1 << 17;
constexpr int WS_AB = 1 << 20;
constexpr int WS_XP = 6 << 20;
constexpr int WS_H = 16 << 20;
constexpr int WS_END = WS_H + 128 * 144 * 1024;
constexpr int HS = 26624;
constexpr int BS = 53248;
constexpr int LDS_PF = 3 * BS;
constexpr int LDS_GW = LDS_PF + 1024;
constexpr int LDS_G = LDS_GW + 2048;
constexpr int LDS_BYTES = LDS_G + 1024;
constexpr int BIMG = 18432;
constexpr int CODE_PF_BYTES = 30208;
constexpr unsigned SPIN_MAX = 1u << 18;
constexpr int AUX_SC1 = 16;
constexpr unsigned POISON = 0xffffffffu;

constexpr int POLL_SLEEP = 2, PF_X = 1, PRIO = 1, AUX_AB = 16, AUX_H = 16, AUX_WS = 0, AUX_W1 = 0, AUX_W2 = 0, AUX_WD = 0, WS_0 = 12, PF_WSN = 8, PF_CL = 8;

struct Params {
  const void* x; const void* wsr; const void* wsi; const void* wdr; const void* wdi;
  const void* w1; const void* w2; const void* gw; void* out; void* ws;
};

__device__ __forceinline__ rsrc_t mk(const void* p, int bytes) {
  return __builtin_amdgcn_make_buffer_rsrc((void*)p, (short)0, bytes, 0x00020000);
}
__device__ __forceinline__ int cslot(int s) {
  int i = s & 3;
  return i == 0 ? 124928 : (i == 1 ? 69632 : (i == 2 ? 0 : 34816));
}
__device__ __forceinline__ unsigned pk(float a, float b) {
  f32x2 v = {a, b};
  return __builtin_bit_cast(unsigned, __builtin_convertvector(v, bf16x2_t));
}
__device__ __forceinline__ float blo(unsigned u) { return __uint_as_float(u << 16); }
__device__ __forceinline__ float bhi(unsigned u) { return __uint_as_float(u & 0xffff0000u); }
__device__ __forceinline__ float rbf(float a) { return blo(pk(a, 0.f)); }
__device__ __forceinline__ bf16x8 ld128(AS3 char* p) { return *(AS3 bf16x8*)p; }
template <int HI = 4608>
__device__ __forceinline__ bf16x8 ldtr(AS3 char* p) {
  s16x4 lo = __builtin_amdgcn_ds_read_tr16_b64_v4i16((AS3 s16x4*)p);
  s16x4 hi = __builtin_amdgcn_ds_read_tr16_b64_v4i16((AS3 s16x4*)(p + HI));
  return __builtin_shufflevector(lo, hi, 0, 1, 2, 3, 4, 5, 6, 7);
}
template <bool DENSE>
__device__ __forceinline__ bf16x8 ldb(AS3 char* b, AS3 char* bt, int n) {
  if (!DENSE) return ldtr<4608>(b + n * 32);
  if (n == 8) return ldtr<512>(bt);
  return ldtr<4096>((AS3 char*)((unsigned)(uintptr_t)b ^ (unsigned)(n << 5)));
}
#define MFMA(a, b, c) __builtin_amdgcn_mfma_f32_16x16x32_bf16(a, b, c, 0, 0, 0)
#define MFMA_ACC(c, a, b) asm volatile("v_mfma_f32_16x16x32_bf16 %0, %1, %2, %0" : "+v"(c) : "v"(a), "v"(b))
#define MFMA_NEW(c, a, b) asm volatile("v_mfma_f32_16x16x32_bf16 %0, %1, %2, 0" : "=&v"(c) : "v"(a), "v"(b))
#define MFMA_Z(c, a, b) do { if (Z) MFMA_NEW(c, a, b); else MFMA_ACC(c, a, b); } while (0)
#define NOP_MFMA_READ() do { SCHED(); asm volatile("s_nop 7\n\ts_nop 3"); SCHED(); } while (0)
#define NOP_VALU_WRITE() do { SCHED(); asm volatile("s_nop 3"); SCHED(); } while (0)
#define BAR() do { asm volatile("" ::: "memory"); __builtin_amdgcn_s_barrier(); asm volatile("" ::: "memory"); } while (0)
#define SCHED() __builtin_amdgcn_sched_barrier(0)
#define WAIT_LGKM0() __builtin_amdgcn_s_waitcnt(0xC07F)
template <int N> __device__ __forceinline__ void wait_vm() {
  asm volatile("s_waitcnt vmcnt(%0)" ::"n"(N) : "memory");
}
template <int AUX = 0>
__device__ __forceinline__ void dma(rsrc_t r, AS3 char* l, int voff, int soff) {
  __builtin_amdgcn_raw_ptr_buffer_load_lds(r, (AS3 void*)l, 16, voff, soff, 0, AUX);
}
template <int NT>
__device__ __forceinline__ void touch(const void* base, int off, unsigned lds_sink) {
  unsigned keep;
  if (NT)
    asm volatile("s_mov_b32 %0, m0\n\ts_mov_b32 m0, %3\n\ts_nop 0\n\tglobal_load_lds_dword %1, %2 nt\n\ts_mov_b32 m0, %0"
                 : "=&s"(keep) : "v"(off), "s"(base), "s"(lds_sink) : "memory");
  else
    asm volatile("s_mov_b32 %0, m0\n\ts_mov_b32 m0, %3\n\ts_nop 0\n\tglobal_load_lds_dword %1, %2\n\ts_mov_b32 m0, %0"
                 : "=&s"(keep) : "v"(off), "s"(base), "s"(lds_sink) : "memory");
}
template <int NT>
__device__ __forceinline__ void touch(const void* base, int, unsigned lds_sink, int off) {
  unsigned keep;
  asm volatile("s_mov_b32 %0, m0\n\ts_mov_b32 m0, %2\n\ts_nop 0\n\tglobal_load_lds_dword %1, off\n\ts_mov_b32 m0, %0"
               : "=&s"(keep) : "v"((const char*)base + off), "s"(lds_sink) : "memory");
}
__device__ __forceinline__ u32x4 swap_pair(const f32x4& a, const f32x4& b) {
  unsigned x0 = pk(a[0], a[1]), x1 = pk(a[2], a[3]), y0 = pk(b[0], b[1]), y1 = pk(b[2], b[3]);
  auto s0 = __builtin_amdgcn_permlane16_swap(x0, y0, false, false);
  auto s1 = __builtin_amdgcn_permlane16_swap(x1, y1, false, false);
  u32x4 v = {s0[0], s1[0], s0[1], s1[1]};
  return v;
}
__device__ __forceinline__ unsigned relu2(unsigned v) {
  typedef __attribute__((ext_vector_type(2))) short s16x2;
  return __builtin_bit_cast(unsigned, __builtin_elementwise_max(__builtin_bit_cast(s16x2, v), s16x2{0, 0}));
}
__device__ __forceinline__ u32x4 relu4(u32x4 v) { return u32x4{relu2(v[0]), relu2(v[1]), relu2(v[2]), relu2(v[3])}; }
__device__ __forceinline__ unsigned poll_issue(const AS1 unsigned* p) {
  unsigned f;
  asm volatile("global_load_dword %0, %1, off sc1" : "=v"(f) : "v"(p) : "memory");
  return f;
}
__device__ __forceinline__ void poll_check(unsigned f, const AS1 unsigned* p, unsigned tag) {
  asm volatile("" : "+v"(f));
  unsigned spins = 0;
#pragma unroll 1
  while (!__all(f == tag)) {
    __builtin_amdgcn_s_sleep(2);
    asm volatile("global_load_dword %0, %1, off sc1\n\ts_waitcnt vmcnt(0)" : "=&v"(f) : "v"(p) : "memory");
    if (++spins > SPIN_MAX) break;
  }
}
__device__ __forceinline__ void flag_store(AS1 unsigned* p, unsigned v) {
  __hip_atomic_store(p, v, __ATOMIC_RELAXED, __HIP_MEMORY_SCOPE_AGENT);
}

#define RSTAMP(i) do {} while (0)
#define STAMP(i) do {} while (0)

__device__ __forceinline__ int rot9(int st, int k) { const int t = st + k; return t >= 9 ? t - 9 : t; }
__device__ __forceinline__ int rot4(int st, int k, int lim) { return st < lim ? (st & ~3) | ((st + k) & 3) : st; }
__device__ __forceinline__ int rot8(int st, int k) { return st < 16 ? (st & 8) | ((st + k) & 7) : st; }
struct Ids { uint64_t pc0; int wg, rho, sig, loc, rb64, pc, rb256, j, rb128, cb, ra; };

#define LBAR() BAR()
template <int NB>
__device__ __forceinline__ void loader(const Params& P, const Ids& I, AS3 char* L, int lw, int lane, unsigned tag) {
  const int wg = I.wg, wave = lw + 4;
  (void)wg; (void)wave;
  constexpr int N1 = 2 + NB, N2 = 8 + NB, N3 = 4 + NB;
  const rsrc_t rx = mk(P.x, S * D * 2);
  const rsrc_t rwd = mk((I.pc >> 2) ? P.wdi : P.wdr, D * D * 2);
  const rsrc_t rw1 = mk(P.w1, D * F * 2);
  const rsrc_t rw2 = mk(P.w2, F * D * 2);
  const rsrc_t rs = mk((I.sig >> 1) ? P.wsi : P.wsr, S * S * 2);
  const rsrc_t rws = mk(P.ws, WS_END);
  const int sw = ((lane & 7) ^ (((lw & 1) << 2) + (lane >> 4))) << 4;
  const int vx = (lw * 8 + (lane >> 3)) * (D * 2) + sw;
  const int vs = (lw * 8 + (lane >> 3)) * (S * 2) + sw;
  const int vl = lane * 16;
  const int v2 = (lw * 8 + (lane >> 4)) * (D * 2) + (((lane >> 1 & 7) ^ ((lw * 4 + (lane >> 4)) & 7)) * 2 + (lane & 1)) * 16;
  int v1[NB];
#pragma unroll
  for (int i = 0; i < NB; ++i) {
    int c = (lw + 4 * i) * 64 + lane, r = c / 18, col = c - r * 18;
    int k = (r & 32) + ((r >> 2) & 3) * 8 + ((r >> 4) & 1) * 4 + (r & 3);
    v1[i] = k * (F * 2) + col * 16;
  }
  auto issueAx = [&](int st) {
    AS3 char* b = L + (st & 3) * HS + lw * 1024;
#pragma unroll
    for (int i = 0; i < 2; ++i) dma(rx, b + i * 4096, vx, (I.rb64 * 64 + i * 32) * (D * 2) + rot9(st, I.ra) * 128);
  };
  auto issueAw = [&](int st) {
    AS3 char* b = L + (st & 3) * HS + lw * 1024;
#pragma unroll
    for (int i = 0; i < NB; ++i) dma<AUX_WD>(rwd, b + 8192 + i * 4096, vx, (i < 4 ? (I.pc & 3) * 128 + i * 32 : 512 + (I.pc & 3) * 16) * (D * 2) + rot9(st, I.ra) * 128);
  };
  auto issueA = [&](int st) { issueAx(st); issueAw(st); };
  auto issueBw = [&](int st) {
    AS3 char* b = L + (2 - st % 3) * BS + lw * 1024;
#pragma unroll
    for (int i = 0; i < NB; ++i) dma<AUX_W1>(rw1, b + 32768 + i * 4096, v1[i], rot4(st, I.rb256, 8) * 64 * (F * 2) + (I.sig * 8 + I.j) * 288);
  };
  auto issueBx = [&](int st) {
    AS3 char* b = L + (2 - st % 3) * BS + lw * 1024;
#pragma unroll
    for (int i = 0; i < 8; ++i) dma(rx, b + i * 4096, vx, (I.rb256 * 256 + i * 32) * (D * 2) + rot4(st, I.rb256, 8) * 128);
  };
  auto issueB = [&](int st) { issueBw(st); issueBx(st); };
  auto issueC = [&](int st) {
    AS3 char* b = L + cslot(st) + lw * 1024;
#pragma unroll
    for (int i = 0; i < 4; ++i) {
      const int so = (I.rb128 * 128 + i * 32) * (S * 2) + (I.sig & 1) * 2048 + rot4(st, I.cb, 16) * 128;
      dma<AUX_WS>(rs, b + i * 4096, vs, so);
    }
#pragma unroll
    for (int i = 0; i < NB; ++i) dma<AUX_AB>(rws, b + 16384 + i * 4096, vl, WS_AB + ((I.sig * 16 + rot4(st, I.cb, 16)) * 4 + I.cb) * BIMG + (lw + 4 * i) * 1024);
  };
  auto issueD = [&](int st) {
    AS3 char* b = L + cslot(st) + lw * 1024;
    const int t = rot8(st - 16, I.rb128);
#pragma unroll
    for (int i = 0; i < 4; ++i)
      dma<AUX_W2>(rw2, b + 16384 + i * 4096, v2, (I.sig * 1152 + t * 64 + (i >> 1) * 32 + (i & 1) * 4) * (D * 2) + I.cb * 256);
    if (NB == 5) dma<AUX_SC1>(rws, b + 32768, vl, WS_W2T + (I.cb * F + I.sig * 1152 + t * 64) * 32 + lw * 1024);
#pragma unroll
    for (int i = 0; i < 4; ++i)
      dma<AUX_H>(rws, b + i * 4096, vl, WS_H + ((I.rb128 * 8 + 2 * i + (lw >> 1)) * 144 + I.sig * 36 + 2 * t + (lw & 1)) * 1024);
  };
  AS1 unsigned* wsu = (AS1 unsigned*)P.ws;

  const unsigned sink = (unsigned)(uintptr_t)(L + LDS_PF) + lw * 256;
  auto pfWs = [&](int st) {
    touch<0>((I.sig >> 1) ? P.wsi : P.wsr,
                            (I.rho * 1024 + I.loc * 32 + (lane >> 1)) * (S * 2) + (I.sig & 1) * 2048 + (st + (lane & 1)) * 128, sink);
  };
  auto pf = [&](int step) {
    if (NB == 4 && lw == 3 && step >= WS_0 && (step - WS_0) * 2 < PF_WSN) pfWs((step - WS_0) * 2);
  };
  auto arm = [&](int i) {
    const u32x4 pz = {POISON, POISON, POISON, POISON};
    __builtin_amdgcn_raw_buffer_store_b128(pz, rws, WS_XP + I.wg * 27648 + min(lw * 7 + i, 26) * 1024 + vl, 0, AUX_SC1);
  };
  STAMP(0); RSTAMP(13);
  if (lw >= 2) dma(mk(P.gw, D * 2), L + LDS_GW + (lw - 2) * 1024, vl + (lw - 2) * 1024, 0);
  int bi = 0; (void)bi;
  issueA(0); issueA(1); issueA(2);
  asm volatile("" ::"s"(tag));
  wait_vm<2 * N1>(); LBAR(); issueA(3);
  STAMP(1);
#pragma unroll 1
  for (int k = 0; k < 5; ++k) { wait_vm<2 * N1>(); LBAR(); issueA(k + 4); }
  wait_vm<2 * N1>(); LBAR(); issueBw(0);
  wait_vm<N1 + NB>(); LBAR(); issueBx(0);
  wait_vm<N2>(); LBAR(); issueBw(1);
  LBAR(); issueBx(1);
  STAMP(2);
  wait_vm<N2>(); LBAR(); issueB(2); pf(10);
  const AS1 unsigned* pfa = wsu + WS_FA / 4 + I.cb * 64 + I.sig * 16 + (lane & 15);
  const AS1 unsigned long long* pg = (const AS1 unsigned long long*)(wsu + WS_G / 4) + I.rb256 * 256 + lw * 64 + lane;
  unsigned f = 0;
  u32x2 gg = {0u, 0u};
#pragma unroll 1
  for (int st = 3; st < 9; ++st) {
    wait_vm<N2>(); LBAR();
    if (st == 5 && lw == 0 && lane < 2)
      flag_store(wsu + (lane ? WS_FW / 4 + I.wg : WS_FA / 4 + (I.pc & 3) * 64 + (I.pc >> 2) * 32 + I.rb64), tag);
    if (st == 8) {
      f = poll_issue(pfa);
      asm volatile("global_load_dwordx2 %0, %1, off sc1" : "=v"(gg) : "v"(pg) : "memory");
    }
    arm(st - 3);
    issueB(st);
    pf(st + 8);
  }
  wait_vm<N2>(); LBAR();
  poll_check(f, pfa, tag);
  arm(6); issueC(0); pf(17);
  {
    asm volatile("" : "+v"(gg));
    unsigned spins = 0;
#pragma unroll 1
    while (!__all(gg[1] == tag)) {
      __builtin_amdgcn_s_sleep(2);
      asm volatile("global_load_dwordx2 %0, %1, off sc1\n\ts_waitcnt vmcnt(0)" : "=&v"(gg) : "v"(pg) : "memory");
      if (++spins > SPIN_MAX) break;
    }
    asm volatile("ds_write_b32 %0, %1\n\ts_waitcnt lgkmcnt(0)" ::"v"((unsigned)(uintptr_t)(L + LDS_G + (lw * 64 + lane) * 4)), "v"(gg[0]) : "memory");
  }
  wait_vm<N3>(); LBAR(); issueC(1); pf(18);
  LBAR(); issueC(2); issueC(3); pf(19);
  STAMP(3);
  wait_vm<0>(); LBAR();
  const AS1 unsigned* pfw = wsu + WS_FW / 4 + I.sig * 64 + lane;
  unsigned f2 = 0;
  const AS1 unsigned* pfh = wsu + WS_FH / 4 + I.rb256 * 32 + ((lane & 8) ? (lane & 3) * 8 + I.j : I.sig * 8 + (lane & 7));
#pragma unroll 1
  for (int u = 0; u < 12; ++u) {
    wait_vm<2 * N3>(); LBAR();
    if (u == 8 && lw == 0 && lane == 0) flag_store(wsu + WS_FH / 4 + I.rb256 * 32 + I.sig * 8 + I.j, tag);
    if (u == 10) { f = poll_issue(pfh); f2 = poll_issue(pfw); }
    issueC(u + 4);
  }
  wait_vm<2 * N3>(); LBAR();
  STAMP(4);
  poll_check(f, pfh, tag);
  poll_check(f2, pfw, tag);
  STAMP(5);
  issueD(16);
#pragma unroll 1
  for (int u = 13; u < 30; ++u) {
    wait_vm<2 * N3>(); LBAR();
    issueD(u + 4);
  }
  wait_vm<2 * N3>(); LBAR();
  wait_vm<N3>(); LBAR();
  wait_vm<0>(); LBAR();
  LBAR();
  STAMP(6);
  if (I.wg == 0 && lw == 1) {
    const AS1 unsigned* pa = wsu + WS_FA / 4 + lane;
#pragma unroll 1
    for (int i = 0; i < 4; ++i) poll_check(0u - (tag == 0u), pa + i * 64, tag);
    if (lane == 0) flag_store(wsu + WS_EPOCH / 4, tag);
  }
  BAR();
  BAR();
  STAMP(7);
}

template <bool DENSE, bool Z = false>
__device__ __forceinline__ void unit(f32x4 (&acc)[4][9], const bf16x8 (&Ac)[4], bf16x8 (&An)[4], bf16x8 (&B)[7],
                                     const bf16x8 (&Xc)[2], bf16x8 (&Xn)[2], AS3 char* sl, int ao, int d2, int bo, int to) {
  SCHED();
  MFMA_Z(acc[0][0], B[0], Ac[0]);
  SCHED();
  AS3 char* a = sl + ao;
  AS3 char* b = sl + bo;
  AS3 char* a2 = a + d2;
  AS3 char* bt = sl + to;
#pragma unroll
  for (int n = 0; n < 7; ++n) {
    unsigned ad = (unsigned)(uintptr_t)b, ax = ad;
    if (n > 0) MFMA_Z(acc[0][n], B[n], Ac[0]);
    SCHED();
    if (n < 4) An[n] = ld128((n < 2 ? a : a2) + (n & 1) * 2048);
    if (DENSE && n == 0) asm volatile("v_xor_b32 %0, 0xe0, %1" : "=v"(ax) : "v"(b));
    SCHED();
    MFMA_Z(acc[1][n], B[n], Ac[1]);
    SCHED();
    if (n < 2) Xn[n] = (DENSE && n == 0) ? ldtr<4096>((AS3 char*)ax) : ldb<DENSE>(b, bt, 7 + n);
    SCHED();
    MFMA_Z(acc[2][n], B[n], Ac[2]);
    SCHED();
    if (DENSE && n > 0) asm volatile("v_xor_b32 %0, %2, %1" : "=v"(ad) : "v"(b), "n"(n << 5));
    SCHED();
    MFMA_Z(acc[3][n], B[n], Ac[3]);
    SCHED();
    B[n] = DENSE ? ldtr<4096>((AS3 char*)ad) : ldb<false>(b, bt, n);
    SCHED();
  }
#pragma unroll
  for (int n = 0; n < 2; ++n)
#pragma unroll
    for (int m = 0; m < 4; ++m) MFMA_Z(acc[m][7 + n], Xc[n], Ac[m]);
  SCHED();
  WAIT_LGKM0();
}
template <bool DENSE>
__device__ __forceinline__ void first_read(bf16x8 (&A)[4], bf16x8 (&B)[7], bf16x8 (&X)[2], AS3 char* a, AS3 char* a2, AS3 char* b, AS3 char* bt) {
#pragma unroll
  for (int m = 0; m < 4; ++m) A[m] = ld128((m < 2 ? a : a2) + (m & 1) * 2048);
#pragma unroll
  for (int n = 0; n < 7; ++n) B[n] = ldb<DENSE>(b, bt, n);
#pragma unroll
  for (int n = 0; n < 2; ++n) X[n] = ldb<DENSE>(b, bt, 7 + n);
  WAIT_LGKM0();
}
template <int ODD>
__device__ __forceinline__ void store_h(const f32x4 (&acc)[4][9], const float (&om)[4], rsrc_t rws, int sbase, int r, int q) {
  const int vp = ((((q & 1) << 1) | (q >> 1)) * 16 + r) * 16 + sbase + ODD * 1024;
  const int vh = ((ODD * 2 + (q >> 1)) * 16 + r) * 16 + (q & 1) * 8 + sbase + (ODD ? 0 : 4096);
#pragma unroll
  for (int m = 0; m < 4; ++m) {
    f32x4 t[9];
#pragma unroll
    for (int n = 0; n < 9; ++n)
#pragma unroll
      for (int e = 0; e < 4; ++e) t[n][e] = acc[m][n][e] * om[m];
#pragma unroll
    for (int p = 0; p < 4; ++p)
      __builtin_amdgcn_raw_buffer_store_b128(relu4(swap_pair(t[2 * p + ODD], t[2 * p + 1 + ODD])), rws,
                                             vp + m * 147456 + p * 1024, 0, AUX_SC1);
    const f32x4 s = t[ODD ? 0 : 8];
    u32x2 v = {relu2(pk(s[0], s[1])), relu2(pk(s[2], s[3]))};
    __builtin_amdgcn_raw_buffer_store_b64(v, rws, vh + m * 147456, 0, AUX_SC1);
  }
}

__device__ __forceinline__ void consumer(const Params& P, const Ids& I, AS3 char* L, int cw, int lane, unsigned tag) {
  const int wg = I.wg, wave = cw;
  (void)wg; (void)wave;
  const int r = lane & 15, q = lane >> 4;
  const rsrc_t rws = mk(P.ws, WS_END);
  if (PRIO) __builtin_amdgcn_s_setprio(PRIO);
  STAMP(0);
  asm volatile("" ::"s"(tag));
  const unsigned sink = (unsigned)(uintptr_t)(L + LDS_PF) + cw * 256;
  if (PF_X && cw == 0) touch<0>(P.x, (I.wg * 72 + lane) * 128, sink);
  if (PF_X && cw == 1)
    touch<0>(lane < 8 ? P.x : (I.wg < 128 ? P.wdr : P.wdi), 0, sink, lane < 8 ? (I.wg * 72 + 64 + lane) * 128 : min((I.wg & 127) * 41 + lane - 8, 5183) * 128);
  const int rc = min(cw * 64 + lane, 143), rf = I.wg * 18 + (rc >> 3);
  const u32x4 rv = *(const u32x4*)((const char*)P.w2 + rf * (D * 2) + 1024 + (rc & 7) * 16);
  const int aL0 = r * 128 + ((q ^ ((r >> 1) & 7)) << 4), aL1 = aL0 ^ 64;
  const int bL = (4 * q + ((lane >> 2) & 3)) * 288 + (lane & 3) * 8;
  f32x4 acc[4][9];
  {
    f32x4 lg = {0.f, 0.f, 0.f, 0.f};
#pragma unroll
    for (int n = 0; n < 9; ++n) { acc[0][n] = f32x4{0.f, 0.f, 0.f, 0.f}; asm volatile("" : "+v"(acc[0][n])); }
    asm volatile("" : "+v"(lg));
#pragma unroll
    for (int m = 1; m < 4; ++m)
#pragma unroll
      for (int n = 0; n < 9; ++n) { acc[m][n] = f32x4{0.f, 0.f, 0.f, 0.f}; asm volatile("" : "+v"(acc[m][n])); }
    bf16x8 xa, xb, w0[10], w1[10];
    BAR();
    asm volatile("" ::"v"(rv));
    STAMP(1);
    xa = ld128(L + aL0 + cw * 2048); xb = ld128(L + aL1 + cw * 2048);
#pragma unroll
    for (int n = 0; n < 9; ++n) { w0[n] = ld128(L + aL0 + 8192 + n * 2048); w1[n] = ld128(L + aL1 + 8192 + n * 2048); }
    w0[9] = ld128(L + LDS_GW + I.ra * 128 + q * 16); w1[9] = ld128(L + LDS_GW + I.ra * 128 + q * 16 + 64);
    WAIT_LGKM0();
    NOP_VALU_WRITE();
    int hs = HS;
#pragma unroll 1
    for (int st = 0; st < 9; ++st) {
      BAR();
      AS3 char* b0 = L + hs + aL0;
      AS3 char* b1 = L + hs + aL1;
      AS3 char* bg = L + LDS_GW + rot9(st + 1, I.ra) * 128 + q * 16;
      SCHED();
#pragma unroll
      for (int n = 0; n < 9; ++n) { MFMA_ACC(acc[0][n], w0[n], xa); SCHED(); w0[n] = ld128(b0 + 8192 + n * 2048); SCHED(); }
      MFMA_ACC(lg, w0[9], xa); SCHED(); w0[9] = ld128(bg); xa = ld128(b0 + cw * 2048); SCHED();
#pragma unroll
      for (int n = 0; n < 9; ++n) { MFMA_ACC(acc[0][n], w1[n], xb); SCHED(); w1[n] = ld128(b1 + 8192 + n * 2048); SCHED(); }
      MFMA_ACC(lg, w1[9], xb); SCHED(); w1[9] = ld128(bg + 64); xb = ld128(b1 + cw * 2048); SCHED();
      WAIT_LGKM0();
      hs = (hs == 3 * HS) ? 0 : hs + HS;
    }
    NOP_MFMA_READ();
    STAMP(2);
    if (cw < 3) {
      const int k = rf & 31, ro = (rf & ~31) + ((k >> 2) & 1) * 16 + (k >> 3) * 4 + (k & 3);
      __builtin_amdgcn_raw_buffer_store_b128(rv, rws, WS_W2T + (((rc & 7) >> 1) * F + ro) * 32 + (rc & 1) * 16, 0, AUX_SC1);
    }
    SCHED();
    if (q == 0 && (r >> 1) == I.pc) {
      const float g = rbf(1.f / (1.f + __expf(-rbf(lg[0]))));
      __hip_atomic_store((AS1 unsigned long long*)((AS1 char*)P.ws + WS_G) + I.rb64 * 64 + cw * 16 + r,
                         ((unsigned long long)tag << 32) | __float_as_uint(g), __ATOMIC_RELAXED, __HIP_MEMORY_SCOPE_AGENT);
    }
    const float sg = (I.pc >> 2) ? -1.f : 1.f;
    const int sl = cw * 16 + r, k = sl & 31;
    const int rho = (sl & 32) + ((k >> 2) & 1) * 16 + (k >> 3) * 4 + (k & 3);
    const int sb = WS_AB + (((I.pc >> 2) * 32 + I.rb64) * 4 + (I.pc & 3)) * BIMG;
    const int vp = rho * 256 + ((q & 1) ^ (rho & 7)) * 32 + (q >> 1) * 16;
#pragma unroll
    for (int p = 0; p < 4; ++p)
      __builtin_amdgcn_raw_buffer_store_b128(swap_pair(acc[0][2 * p] * sg, acc[0][2 * p + 1] * sg), rws, sb + (vp ^ (p * 64)), 0, AUX_SC1);
    const f32x4 s = acc[0][8] * sg;
    u32x2 v = {pk(s[0], s[1]), pk(s[2], s[3])};
    __builtin_amdgcn_raw_buffer_store_b64(v, rws, sb + 16384 + rho * 32 + q * 8, 0, AUX_SC1);
  }
  const int mh = cw >> 1, kg = cw & 1;
  bf16x8 A0[4], A1[4], B[7], X0[2], X1[2];
  {
#pragma unroll
    for (int n = 0; n < 9; ++n) { acc[0][n] = f32x4{0.f, 0.f, 0.f, 0.f}; asm volatile("" : "+v"(acc[0][n])); }
    const int a0 = aL0 + cw * 8192, a1 = aL1 + cw * 8192, b0 = bL + 32768;
    BAR();
    STAMP(3);
    int sl = 2 * BS;
    first_read<false>(A0, B, X0, L + sl + a0, L + sl + a0 + 4096, L + sl + b0, L);
    NOP_VALU_WRITE();
#pragma unroll 1
    for (int st = 0; st < 9; ++st) {
      if (st == 2) wait_vm<0>();
      unit<false>(acc, A0, A1, B, X0, X1, L + sl, a1, 4096, b0 + 9216, 0);
      BAR();
      sl = (sl == 0) ? 2 * BS : sl - BS;
      unit<false>(acc, A1, A0, B, X1, X0, L + sl, a0, 4096, b0, 0);
    }
    NOP_MFMA_READ();
    STAMP(4);
    float om[4];
#pragma unroll
    for (int m = 0; m < 4; ++m) {
      om[m] = rbf(1.f - *(AS3 float*)(L + LDS_G + (cw * 64 + m * 16 + r) * 4));
    }
    const int sb = WS_H + ((I.rb256 * 16 + cw * 4) * 144 + I.sig * 36 + ((I.j * 9) >> 1)) * 1024;
    if (I.j & 1) store_h<1>(acc, om, rws, sb, r, q);
    else store_h<0>(acc, om, rws, sb, r, q);
    STAMP(5);
  }
  {
    asm volatile("" : "+v"(lane));
    const int aS = (kg ? aL1 : aL0) + mh * 8192 + kg * 4096, aH = mh * 8192 + kg * 5120 + lane * 16;
    const int d2 = kg ? -4096 : 4096;
    const int br = kg * 32 + (lane >> 2 & 15);
    const int bb = 16384 + br * 256 + (br & 7) * 32 + (lane & 3) * 8, bt = 32768 + br * 32 + (lane & 3) * 8;
    BAR();
    STAMP(6);
    first_read<true>(A0, B, X0, L + cslot(0) + aS, L + cslot(0) + aS + d2, L + cslot(0) + bb, L + cslot(0) + bt);
    BAR();
    unit<true, true>(acc, A0, A1, B, X0, X1, L + cslot(1), aS, d2, bb, bt);
    BAR();
    unit<true>(acc, A1, A0, B, X1, X0, L + cslot(2), aS, d2, bb, bt);
#pragma unroll 1
    for (int u = 2; u < 16; u += 2) {
      if (u == 8) wait_vm<0>();
      const int s1 = cslot(u + 1), s2 = cslot(u + 2), an = u < 14 ? aS : aH;
      asm volatile("" ::"s"(s1), "s"(s2), "v"(an));
      BAR();
      unit<true>(acc, A0, A1, B, X0, X1, L + s1, aS, d2, bb, bt);
      BAR();
      unit<true>(acc, A1, A0, B, X1, X0, L + s2, an, d2, bb, bt);
    }
    NOP_MFMA_READ();
    STAMP(7);
#pragma unroll
    for (int m = 0; m < 4; ++m) {
      const float g = *(AS3 float*)(L + LDS_G + ((I.j & 1) * 128 + mh * 64 + (m ^ (2 * kg)) * 16 + (lane & 15)) * 4);
#pragma unroll
      for (int n = 0; n < 9; ++n) { acc[m][n] *= g; asm volatile("" : "+v"(acc[m][n])); }
    }
    NOP_VALU_WRITE();
#pragma unroll 1
    for (int u = 16; u < 34; u += 2) {
      const int s1 = cslot(u + 1), s2 = cslot(u + 2);
      asm volatile("" ::"s"(s1), "s"(s2));
      BAR();
      unit<true>(acc, A0, A1, B, X0, X1, L + s1, aH, d2, bb, bt);
      BAR();
      unit<true>(acc, A1, A0, B, X1, X0, L + s2, aH, d2, bb, bt);
    }
    NOP_MFMA_READ();
    STAMP(8);
  }
  {
    asm volatile("" : "+v"(lane));
    AS3 char* wr = L + cw * BIMG + lane * 16;
    AS3 char* rd = L + (cw ^ 1) * BIMG + lane * 16;
#pragma unroll
    for (int m = 0; m < 2; ++m)
#pragma unroll
      for (int n = 0; n < 9; ++n) *(AS3 f32x4*)(wr + (m * 9 + n) * 1024) = acc[2 + m][n];
    WAIT_LGKM0();
    BAR();
    STAMP(10);
#pragma unroll
    for (int m = 0; m < 2; ++m)
#pragma unroll
      for (int n = 0; n < 9; ++n) acc[m][n] += *(AS3 f32x4*)(rd + (m * 9 + n) * 1024);
    if (cw == I.sig) {
      AS3 char* kp = L + 4 * BIMG + lane * 16;
#pragma unroll
      for (int m = 0; m < 2; ++m) {
#pragma unroll
        for (int n = 0; n < 8; ++n) *(AS3 f32x4*)(kp + (m * 8 + n) * 1024) = acc[m][n];
        *(AS3 f32x4*)(kp + (16 + m) * 1024) = acc[m][8];
      }
      WAIT_LGKM0();
    } else {
      const int sb = WS_XP + ((I.loc * 8 + I.rho * 4 + cw) * 3 + ((I.sig - cw - 1) & 3)) * 9216 + lane * 16;
#pragma unroll
      for (int it = 0; it < 9; ++it) {
        const f32x4 a = it < 8 ? acc[it >> 2][(it & 3) * 2] : acc[0][8];
        const f32x4 b = it < 8 ? acc[it >> 2][(it & 3) * 2 + 1] : acc[1][8];
        u32x4 v = {min(pk(a[0], a[1]), POISON - 1), min(pk(a[2], a[3]), POISON - 1), min(pk(b[0], b[1]), POISON - 1), min(pk(b[2], b[3]), POISON - 1)};
        __builtin_amdgcn_raw_buffer_store_b128(v, rws, sb + it * 1024, 0, AUX_SC1);
      }
    }
    STAMP(9);
    BAR();
  }
}

__device__ __forceinline__ void final_items(const Params& P, const Ids& I, AS3 char* L, int w, int lane) {
  const rsrc_t rws = mk(P.ws, WS_END);
  const rsrc_t ro = mk(P.out, S * D * 2);
  const int r = lane & 15, q = lane >> 4;
  const int vb = WS_XP + I.wg * 27648 + lane * 16;
  f32x4 a = *(AS3 f32x4*)(L + 4 * BIMG + w * 2048 + lane * 16);
  f32x4 b = *(AS3 f32x4*)(L + 4 * BIMG + w * 2048 + 1024 + lane * 16);
  f32x4 c = *(AS3 f32x4*)(L + 4 * BIMG + 16384 + (w & 1) * 1024 + lane * 16);
  u32x4 p[3];
  u32x2 e[3];
#pragma unroll
  for (int i = 0; i < 3; ++i) { e[i] = u32x2{0u, 0u}; p[i] = u32x4{0u, 0u, 0u, 0u}; }
  unsigned todo = 0x7;
#pragma unroll 1
  for (unsigned spins = 0; todo && spins < SPIN_MAX; ++spins) {
#pragma unroll
    for (int i = 0; i < 3; ++i)
      if (todo >> i & 1) {
        p[i] = __builtin_amdgcn_raw_buffer_load_b128(rws, vb + i * 9216 + w * 1024, 0, AUX_SC1);
        if (w < 2) e[i] = __builtin_amdgcn_raw_buffer_load_b64(rws, vb + i * 9216 + 8192 + w * 8, 0, AUX_SC1);
      }
#pragma unroll
    for (int i = 0; i < 3; ++i)
      if (todo >> i & 1) {
        const unsigned mx = max(max(max(p[i][0], p[i][1]), max(p[i][2], p[i][3])), max(e[i][0], e[i][1]));
        if (__all(mx != POISON)) todo &= ~(1u << i);
      }
    if (todo) __builtin_amdgcn_s_sleep(POLL_SLEEP);
  }
#pragma unroll
  for (int i = 0; i < 3; ++i) {
    a += f32x4{blo(p[i][0]), bhi(p[i][0]), blo(p[i][1]), bhi(p[i][1])};
    b += f32x4{blo(p[i][2]), bhi(p[i][2]), blo(p[i][3]), bhi(p[i][3])};
    c += f32x4{blo(e[i][0]), bhi(e[i][0]), blo(e[i][1]), bhi(e[i][1])};
  }
  const int row0 = I.rb128 * 128 + I.sig * 32, col0 = I.cb * 128;
  __builtin_amdgcn_raw_buffer_store_b128(swap_pair(a, b), ro, (row0 + (w >> 2) * 16 + r) * (D * 2) + (col0 + (w & 3) * 32) * 2 + (((q & 1) << 1) | (q >> 1)) * 16, 0, AUX_SC1);
  if (w < 2) {
    u32x2 v = {pk(c[0], c[1]), pk(c[2], c[3])};
    __builtin_amdgcn_raw_buffer_store_b64(v, ro, (row0 + w * 16 + r) * (D * 2) + (512 + I.cb * 16) * 2 + q * 8, 0, AUX_SC1);
  }
}
}

__global__ __launch_bounds__(512, 2) void gated_mix_fnet_ffn_kernel(Params P) {
  __shared__ __attribute__((aligned(1024))) char lds[LDS_BYTES];
  AS3 char* L = (AS3 char*)lds;
  const int lane = threadIdx.x & 63;
  const int wave = __builtin_amdgcn_readfirstlane(threadIdx.x >> 6);
  Ids I;
  asm volatile("s_getpc_b64 %0" : "=s"(I.pc0));
  I.wg = blockIdx.x;
  if (wave == 0)
    touch<0>((const void*)I.pc0, min(((I.wg >> 3) * PF_CL + (lane & (PF_CL - 1))) * 128, CODE_PF_BYTES), (unsigned)(uintptr_t)(L + LDS_PF));
  I.rho = (I.wg >> 2) & 1; I.sig = I.wg & 3; I.loc = I.wg >> 3;
  I.rb64 = 16 * I.rho + (I.loc >> 1); I.pc = 2 * I.sig + (I.loc & 1);
  I.rb256 = 4 * I.rho + (I.loc >> 3); I.j = I.loc & 7;
  I.rb128 = 2 * I.rb256 + (I.j & 1); I.cb = I.j >> 1;
  I.ra = (I.rb64 & 15) % 9;
  const unsigned tag = *(const __attribute__((address_space(4))) unsigned*)((uintptr_t)P.ws + WS_EPOCH) + 1u;
  if (wave < 4) consumer(P, I, L, wave, lane, tag);
  else if (wave < 6) loader<5>(P, I, L, wave - 4, lane, tag);
  else loader<4>(P, I, L, wave - 4, lane, tag);
  {
    int tz = threadIdx.x;
    asm volatile("" : "+v"(tz));
    const int w2 = __builtin_amdgcn_readfirstlane(tz >> 6);
    final_items(P, I, L, w2, tz & 63);
  }
}

extern "C" void kernel_launch(void* const* d_in, const int* in_sizes, int n_in, void* d_out, int out_size, void* d_ws,
                              size_t ws_size, hipStream_t stream) {
  Params P;
  P.x = d_in[0]; P.wsr = d_in[1]; P.wsi = d_in[2]; P.wdr = d_in[3]; P.wdi = d_in[4];
  P.w1 = d_in[6]; P.w2 = d_in[7]; P.gw = d_in[8]; P.out = d_out; P.ws = d_ws;
  gated_mix_fnet_ffn_kernel<<<256, 512, 0, stream>>>(P);
}
```

```cpp
#include <hip/hip_runtime.h>
#include <hip/hip_bf16.h>
#include <stdint.h>

typedef __attribute__((ext_vector_type(8))) short bf16x8;
typedef __attribute__((ext_vector_type(4))) short s16x4;
typedef __attribute__((ext_vector_type(4))) float f32x4;
typedef __attribute__((ext_vector_type(2))) float f32x2;
typedef __attribute__((ext_vector_type(4))) unsigned u32x4;
typedef __attribute__((ext_vector_type(2))) unsigned u32x2;
typedef __attribute__((ext_vector_type(2))) __bf16 bf16x2_t;
#define AS3 __attribute__((address_space(3)))
#define AS1 __attribute__((address_space(1)))
typedef __amdgpu_buffer_rsrc_t rsrc_t;

namespace {
constexpr int S = 2048, D = 576, F = 4608;
constexpr int WS_EPOCH = 0;
constexpr int WS_FA = 4096;
constexpr int WS_FH = 8192;
constexpr int WS_FW = 12288;
constexpr int WS_G = 32768;
constexpr int WS_PROF = 64 << 20;
constexpr int WS_W2T = 1 << 17;
constexpr int WS_AB = 1 << 20;
constexpr int WS_XP = 6 << 20;
constexpr int WS_H = 16 << 20;
constexpr int WS_END = WS_H + 128 * 144 * 1024;
constexpr int HS = 26624;
constexpr int BS = 53248;
constexpr int LDS_PF = 3 * BS;
constexpr int LDS_GW = LDS_PF + 1024;
constexpr int LDS_G = LDS_GW + 2048;
constexpr int LDS_BYTES = LDS_G + 1024;
constexpr int BIMG = 18432;
constexpr int CODE_PF_BYTES = 30208;
constexpr unsigned SPIN_MAX = 1u << 18;
constexpr int AUX_SC1 = 16;
constexpr unsigned POISON = 0xffffffffu;

constexpr int POLL_SLEEP = 2, PF_X = 1, PRIO = 1, AUX_AB = 16, AUX_H = 16, AUX_WS = 0, AUX_W1 = 0, AUX_W2 = 0, AUX_WD = 0, WS_0 = 12, PF_WSN = 8, PF_CL = 8;

struct Params {
  const void* x; const void* wsr; const void* wsi; const void* wdr; const void* wdi;
  const void* w1; const void* w2; const void* gw; void* out; void* ws;
};

__device__ __forceinline__ rsrc_t mk(const void* p, int bytes) {
  return __builtin_amdgcn_make_buffer_rsrc((void*)p, (short)0, bytes, 0x00020000);
}
__device__ __forceinline__ int cslot(int s) {
  int i = s & 3;
  return i == 0 ? 124928 : (i == 1 ? 69632 : (i == 2 ? 0 : 34816));
}
__device__ __forceinline__ unsigned pk(float a, float b) {
  f32x2 v = {a, b};
  return __builtin_bit_cast(unsigned, __builtin_convertvector(v, bf16x2_t));
}
__device__ __forceinline__ float blo(unsigned u) { return __uint_as_float(u << 16); }
__device__ __forceinline__ float bhi(unsigned u) { return __uint_as_float(u & 0xffff0000u); }
__device__ __forceinline__ float rbf(float a) { return blo(pk(a, 0.f)); }
__device__ __forceinline__ bf16x8 ld128(AS3 char* p) { return *(AS3 bf16x8*)p; }
template <int HI = 4608>
__device__ __forceinline__ bf16x8 ldtr(AS3 char* p) {
  s16x4 lo = __builtin_amdgcn_ds_read_tr16_b64_v4i16((AS3 s16x4*)p);
  s16x4 hi = __builtin_amdgcn_ds_read_tr16_b64_v4i16((AS3 s16x4*)(p + HI));
  return __builtin_shufflevector(lo, hi, 0, 1, 2, 3, 4, 5, 6, 7);
}
template <bool DENSE>
__device__ __forceinline__ bf16x8 ldb(AS3 char* b, AS3 char* bt, int n) {
  if (!DENSE) return ldtr<4608>(b + n * 32);
  if (n == 8) return ldtr<512>(bt);
  return ldtr<4096>((AS3 char*)((unsigned)(uintptr_t)b ^ (unsigned)(n << 5)));
}
#define MFMA(a, b, c) __builtin_amdgcn_mfma_f32_16x16x32_bf16(a, b, c, 0, 0, 0)
#define MFMA_ACC(c, a, b) asm volatile("v_mfma_f32_16x16x32_bf16 %0, %1, %2, %0" : "+v"(c) : "v"(a), "v"(b))
#define MFMA_NEW(c, a, b) asm volatile("v_mfma_f32_16x16x32_bf16 %0, %1, %2, 0" : "=&v"(c) : "v"(a), "v"(b))
#define MFMA_Z(c, a, b) do { if (Z) MFMA_NEW(c, a, b); else MFMA_ACC(c, a, b); } while (0)
#define NOP_MFMA_READ() do { SCHED(); asm volatile("s_nop 7\n\ts_nop 3"); SCHED(); } while (0)
#define NOP_VALU_WRITE() do { SCHED(); asm volatile("s_nop 3"); SCHED(); } while (0)
#define BAR() do { asm volatile("" ::: "memory"); __builtin_amdgcn_s_barrier(); asm volatile("" ::: "memory"); } while (0)
#define SCHED() __builtin_amdgcn_sched_barrier(0)
#define WAIT_LGKM0() __builtin_amdgcn_s_waitcnt(0xC07F)
template <int N> __device__ __forceinline__ void wait_vm() {
  asm volatile("s_waitcnt vmcnt(%0)" ::"n"(N) : "memory");
}
template <int AUX = 0>
__device__ __forceinline__ void dma(rsrc_t r, AS3 char* l, int voff, int soff) {
  __builtin_amdgcn_raw_ptr_buffer_load_lds(r, (AS3 void*)l, 16, voff, soff, 0, AUX);
}
template <int NT>
__device__ __forceinline__ void touch(const void* base, int off, unsigned lds_sink) {
  unsigned keep;
  if (NT)
    asm volatile("s_mov_b32 %0, m0\n\ts_mov_b32 m0, %3\n\ts_nop 0\n\tglobal_load_lds_dword %1, %2 nt\n\ts_mov_b32 m0, %0"
                 : "=&s"(keep) : "v"(off), "s"(base), "s"(lds_sink) : "memory");
  else
    asm volatile("s_mov_b32 %0, m0\n\ts_mov_b32 m0, %3\n\ts_nop 0\n\tglobal_load_lds_dword %1, %2\n\ts_mov_b32 m0, %0"
                 : "=&s"(keep) : "v"(off), "s"(base), "s"(lds_sink) : "memory");
}
template <int NT>
__device__ __forceinline__ void touch(const void* base, int, unsigned lds_sink, int off) {
  unsigned keep;
  asm volatile("s_mov_b32 %0, m0\n\ts_mov_b32 m0, %2\n\ts_nop 0\n\tglobal_load_lds_dword %1, off\n\ts_mov_b32 m0, %0"
               : "=&s"(keep) : "v"((const char*)base + off), "s"(lds_sink) : "memory");
}
__device__ __forceinline__ u32x4 swap_pair(const f32x4& a, const f32x4& b) {
  unsigned x0 = pk(a[0], a[1]), x1 = pk(a[2], a[3]), y0 = pk(b[0], b[1]), y1 = pk(b[2], b[3]);
  auto s0 = __builtin_amdgcn_permlane16_swap(x0, y0, false, false);
  auto s1 = __builtin_amdgcn_permlane16_swap(x1, y1, false, false);
  u32x4 v = {s0[0], s1[0], s0[1], s1[1]};
  return v;
}
__device__ __forceinline__ unsigned relu2(unsigned v) {
  typedef __attribute__((ext_vector_type(2))) short s16x2;
  return __builtin_bit_cast(unsigned, __builtin_elementwise_max(__builtin_bit_cast(s16x2, v), s16x2{0, 0}));
}
__device__ __forceinline__ u32x4 relu4(u32x4 v) { return u32x4{relu2(v[0]), relu2(v[1]), relu2(v[2]), relu2(v[3])}; }
__device__ __forceinline__ unsigned poll_issue(const AS1 unsigned* p) {
  unsigned f;
  asm volatile("global_load_dword %0, %1, off sc1" : "=v"(f) : "v"(p) : "memory");
  return f;
}
__device__ __forceinline__ void poll_check(unsigned f, const AS1 unsigned* p, unsigned tag) {
  asm volatile("" : "+v"(f));
  unsigned spins = 0;
#pragma unroll 1
  while (!__all(f == tag)) {
    __builtin_amdgcn_s_sleep(2);
    asm volatile("global_load_dword %0, %1, off sc1\n\ts_waitcnt vmcnt(0)" : "=&v"(f) : "v"(p) : "memory");
    if (++spins > SPIN_MAX) break;
  }
}
__device__ __forceinline__ void flag_store(AS1 unsigned* p, unsigned v) {
  __hip_atomic_store(p, v, __ATOMIC_RELAXED, __HIP_MEMORY_SCOPE_AGENT);
}

#define RSTAMP(i) do {} while (0)
#define STAMP(i) do {} while (0)

__device__ __forceinline__ int rot9(int st, int k) { const int t = st + k; return t >= 9 ? t - 9 : t; }
__device__ __forceinline__ int rot4(int st, int k, int lim) { return st < lim ? (st & ~3) | ((st + k) & 3) : st; }
__device__ __forceinline__ int rot8(int st, int k) { return st < 16 ? (st & 8) | ((st + k) & 7) : st; }
struct Ids { uint64_t pc0; int wg, rho, sig, loc, rb64, pc, rb256, j, rb128, cb, ra; };

#define LBAR() BAR()
template <int NB>
__device__ __forceinline__ void loader(const Params& P, const Ids& I, AS3 char* L, int lw, int lane, unsigned tag) {
  const int wg = I.wg, wave = lw + 4;
  (void)wg; (void)wave;
  constexpr int N1 = 2 + NB, N2 = 8 + NB, N3 = 4 + NB;
  const rsrc_t rx = mk(P.x, S * D * 2);
  const rsrc_t rwd = mk((I.pc >> 2) ? P.wdi : P.wdr, D * D * 2);
  const rsrc_t rw1 = mk(P.w1, D * F * 2);
  const rsrc_t rw2 = mk(P.w2, F * D * 2);
  const rsrc_t rs = mk((I.sig >> 1) ? P.wsi : P.wsr, S * S * 2);
  const rsrc_t rws = mk(P.ws, WS_END);
  const int sw = ((lane & 7) ^ (((lw & 1) << 2) + (lane >> 4))) << 4;
  const int vx = (lw * 8 + (lane >> 3)) * (D * 2) + sw;
  const int vs = (lw * 8 + (lane >> 3)) * (S * 2) + sw;
  const int vl = lane * 16;
  const int v2 = (lw * 8 + (lane >> 4)) * (D * 2) + (((lane >> 1 & 7) ^ ((lw * 4 + (lane >> 4)) & 7)) * 2 + (lane & 1)) * 16;
  int v1[NB];
#pragma unroll
  for (int i = 0; i < NB; ++i) {
    int c = (lw + 4 * i) * 64 + lane, r = c / 18, col = c - r * 18;
    int k = (r & 32) + ((r >> 2) & 3) * 8 + ((r >> 4) & 1) * 4 + (r & 3);
    v1[i] = k * (F * 2) + col * 16;
  }
  auto issueAx = [&](int st) {
    AS3 char* b = L + (st & 3) * HS + lw * 1024;
#pragma unroll
    for (int i = 0; i < 2; ++i) dma(rx, b + i * 4096, vx, (I.rb64 * 64 + i * 32) * (D * 2) + rot9(st, I.ra) * 128);
  };
  auto issueAw = [&](int st) {
    AS3 char* b = L + (st & 3) * HS + lw * 1024;
#pragma unroll
    for (int i = 0; i < NB; ++i) dma<AUX_WD>(rwd, b + 8192 + i * 4096, vx, (i < 4 ? (I.pc & 3) * 128 + i * 32 : 512 + (I.pc & 3) * 16) * (D * 2) + rot9(st, I.ra) * 128);
  };
  auto issueA = [&](int st) { issueAx(st); issueAw(st); };
  auto issueBw = [&](int st) {
    AS3 char* b = L + (2 - st % 3) * BS + lw * 1024;
#pragma unroll
    for (int i = 0; i < NB; ++i) dma<AUX_W1>(rw1, b + 32768 + i * 4096, v1[i], rot4(st, I.rb256, 8) * 64 * (F * 2) + (I.sig * 8 + I.j) * 288);
  };
  auto issueBx = [&](int st) {
    AS3 char* b = L + (2 - st % 3) * BS + lw * 1024;
#pragma unroll
    for (int i = 0; i < 8; ++i) dma(rx, b + i * 4096, vx, (I.rb256 * 256 + i * 32) * (D * 2) + rot4(st, I.rb256, 8) * 128);
  };
  auto issueB = [&](int st) { issueBw(st); issueBx(st); };
  auto issueC = [&](int st) {
    AS3 char* b = L + cslot(st) + lw * 1024;
#pragma unroll
    for (int i = 0; i < 4; ++i) {
      const int so = (I.rb128 * 128 + i * 32) * (S * 2) + (I.sig & 1) * 2048 + rot4(st, I.cb, 16) * 128;
      dma<AUX_WS>(rs, b + i * 4096, vs, so);
    }
#pragma unroll
    for (int i = 0; i < NB; ++i) dma<AUX_AB>(rws, b + 16384 + i * 4096, vl, WS_AB + ((I.sig * 16 + rot4(st, I.cb, 16)) * 4 + I.cb) * BIMG + (lw + 4 * i) * 1024);
  };
  auto issueD = [&](int st) {
    AS3 char* b = L + cslot(st) + lw * 1024;
    const int t = rot8(st - 16, I.rb128);
#pragma unroll
    for (int i = 0; i < 4; ++i)
      dma<AUX_W2>(rw2, b + 16384 + i * 4096, v2, (I.sig * 1152 + t * 64 + (i >> 1) * 32 + (i & 1) * 4) * (D * 2) + I.cb * 256);
    if (NB == 5) dma<AUX_SC1>(rws, b + 32768, vl, WS_W2T + (I.cb * F + I.sig * 1152 + t * 64) * 32 + lw * 1024);
#pragma unroll
    for (int i = 0; i < 4; ++i)
      dma<AUX_H>(rws, b + i * 4096, vl, WS_H + ((I.rb128 * 8 + 2 * i + (lw >> 1)) * 144 + I.sig * 36 + 2 * t + (lw & 1)) * 1024);
  };
  AS1 unsigned* wsu = (AS1 unsigned*)P.ws;

  const unsigned sink = (unsigned)(uintptr_t)(L + LDS_PF) + lw * 256;
  auto pfWs = [&](int st) {
    touch<0>((I.sig >> 1) ? P.wsi : P.wsr,
                            (I.rho * 1024 + I.loc * 32 + (lane >> 1)) * (S * 2) + (I.sig & 1) * 2048 + (st + (lane & 1)) * 128, sink);
  };
  auto pf = [&](int step) {
    if (NB == 4 && lw == 3 && step >= WS_0 && (step - WS_0) * 2 < PF_WSN) pfWs((step - WS_0) * 2);
  };
  auto arm = [&](int i) {
    const u32x4 pz = {POISON, POISON, POISON, POISON};
    __builtin_amdgcn_raw_buffer_store_b128(pz, rws, WS_XP + I.wg * 27648 + min(lw * 7 + i, 26) * 1024 + vl, 0, AUX_SC1);
  };
  STAMP(0); RSTAMP(13);
  if (lw >= 2) dma(mk(P.gw, D * 2), L + LDS_GW + (lw - 2) * 1024, vl + (lw - 2) * 1024, 0);
  int bi = 0; (void)bi;
  issueA(0); issueA(1); issueA(2);
  asm volatile("" : "+s"(tag));
  wait_vm<2 * N1>(); LBAR(); issueA(3);
  STAMP(1);
#pragma unroll 1
  for (int k = 0; k < 5; ++k) { wait_vm<2 * N1>(); LBAR(); issueA(k + 4); }
  wait_vm<2 * N1>(); LBAR(); issueBw(0);
  wait_vm<N1 + NB>(); LBAR(); issueBx(0);
  wait_vm<N2>(); LBAR(); issueBw(1);
  LBAR(); issueBx(1);
  STAMP(2);
  wait_vm<N2>(); LBAR(); issueB(2); pf(10);
  const AS1 unsigned* pfa = wsu + WS_FA / 4 + I.cb * 64 + I.sig * 16 + (lane & 15);
  const AS1 unsigned long long* pg = (const AS1 unsigned long long*)(wsu + WS_G / 4) + I.rb256 * 256 + lw * 64 + lane;
  unsigned f = 0;
  u32x2 gg = {0u, 0u};
#pragma unroll 1
  for (int st = 3; st < 9; ++st) {
    wait_vm<N2>(); LBAR();
    if (st == 5 && lw == 0 && lane < 2)
      flag_store(wsu + (lane ? WS_FW / 4 + I.wg : WS_FA / 4 + (I.pc & 3) * 64 + (I.pc >> 2) * 32 + I.rb64), tag);
    if (st == 8) {
      f = poll_issue(pfa);
      asm volatile("global_load_dwordx2 %0, %1, off sc1" : "=v"(gg) : "v"(pg) : "memory");
    }
    arm(st - 3);
    issueB(st);
    pf(st + 8);
  }
  wait_vm<N2>(); LBAR();
  poll_check(f, pfa, tag);
  arm(6); issueC(0); pf(17);
  {
    asm volatile("" : "+v"(gg));
    unsigned spins = 0;
#pragma unroll 1
    while (!__all(gg[1] == tag)) {
      __builtin_amdgcn_s_sleep(2);
      asm volatile("global_load_dwordx2 %0, %1, off sc1\n\ts_waitcnt vmcnt(0)" : "=&v"(gg) : "v"(pg) : "memory");
      if (++spins > SPIN_MAX) break;
    }
    asm volatile("ds_write_b32 %0, %1\n\ts_waitcnt lgkmcnt(0)" ::"v"((unsigned)(uintptr_t)(L + LDS_G + (lw * 64 + lane) * 4)), "v"(gg[0]) : "memory");
  }
  wait_vm<N3>(); LBAR(); issueC(1); pf(18);
  LBAR(); issueC(2); issueC(3); pf(19);
  STAMP(3);
  wait_vm<0>(); LBAR();
  const AS1 unsigned* pfw = wsu + WS_FW / 4 + I.sig * 64 + lane;
  unsigned f2 = 0;
  const AS1 unsigned* pfh = wsu + WS_FH / 4 + I.rb256 * 32 + ((lane & 8) ? (lane & 3) * 8 + I.j : I.sig * 8 + (lane & 7));
#pragma unroll 1
  for (int u = 0; u < 12; ++u) {
    wait_vm<2 * N3>(); LBAR();
    if (u == 8 && lw == 0 && lane == 0) flag_store(wsu + WS_FH / 4 + I.rb256 * 32 + I.sig * 8 + I.j, tag);
    if (u == 10) { f = poll_issue(pfh); f2 = poll_issue(pfw); }
    issueC(u + 4);
  }
  wait_vm<2 * N3>(); LBAR();
  STAMP(4);
  poll_check(f, pfh, tag);
  poll_check(f2, pfw, tag);
  STAMP(5);
  issueD(16);
#pragma unroll 1
  for (int u = 13; u < 30; ++u) {
    wait_vm<2 * N3>(); LBAR();
    issueD(u + 4);
  }
  wait_vm<2 * N3>(); LBAR();
  wait_vm<N3>(); LBAR();
  wait_vm<0>(); LBAR();
  LBAR();
  STAMP(6);
  if (I.wg == 0 && lw == 1) {
    const AS1 unsigned* pa = wsu + WS_FA / 4 + lane;
#pragma unroll 1
    for (int i = 0; i < 4; ++i) poll_check(0u - (tag == 0u), pa + i * 64, tag);
    if (lane == 0) flag_store(wsu + WS_EPOCH / 4, tag);
  }
  BAR();
  BAR();
  STAMP(7);
}

template <bool DENSE, bool Z = false>
__device__ __forceinline__ void unit(f32x4 (&acc)[4][9], const bf16x8 (&Ac)[4], bf16x8 (&An)[4], bf16x8 (&B)[7],
                                     const bf16x8 (&Xc)[2], bf16x8 (&Xn)[2], AS3 char* sl, int ao, int d2, int bo, int to) {
  SCHED();
  MFMA_Z(acc[0][0], B[0], Ac[0]);
  SCHED();
  AS3 char* a = sl + ao;
  AS3 char* b = sl + bo;
  AS3 char* a2 = a + d2;
  AS3 char* bt = sl + to;
#pragma unroll
  for (int n = 0; n < 7; ++n) {
    unsigned ad = (unsigned)(uintptr_t)b, ax = ad;
    if (n > 0) MFMA_Z(acc[0][n], B[n], Ac[0]);
    SCHED();
    if (n < 4) An[n] = ld128((n < 2 ? a : a2) + (n & 1) * 2048);
    if (DENSE && n == 0) asm volatile("v_xor_b32 %0, 0xe0, %1" : "=v"(ax) : "v"(b));
    SCHED();
    MFMA_Z(acc[1][n], B[n], Ac[1]);
    SCHED();
    if (n < 2) Xn[n] = (DENSE && n == 0) ? ldtr<4096>((AS3 char*)ax) : ldb<DENSE>(b, bt, 7 + n);
    SCHED();
    MFMA_Z(acc[2][n], B[n], Ac[2]);
    SCHED();
    if (DENSE && n > 0) asm volatile("v_xor_b32 %0, %2, %1" : "=v"(ad) : "v"(b), "n"(n << 5));
    SCHED();
    MFMA_Z(acc[3][n], B[n], Ac[3]);
    SCHED();
    B[n] = DENSE ? ldtr<4096>((AS3 char*)ad) : ldb<false>(b, bt, n);
    SCHED();
  }
#pragma unroll
  for (int n = 0; n < 2; ++n)
#pragma unroll
    for (int m = 0; m < 4; ++m) MFMA_Z(acc[m][7 + n], Xc[n], Ac[m]);
  SCHED();
  WAIT_LGKM0();
}
template <bool DENSE>
__device__ __forceinline__ void first_read(bf16x8 (&A)[4], bf16x8 (&B)[7], bf16x8 (&X)[2], AS3 char* a, AS3 char* a2, AS3 char* b, AS3 char* bt) {
#pragma unroll
  for (int m = 0; m < 4; ++m) A[m] = ld128((m < 2 ? a : a2) + (m & 1) * 2048);
#pragma unroll
  for (int n = 0; n < 7; ++n) B[n] = ldb<DENSE>(b, bt, n);
#pragma unroll
  for (int n = 0; n < 2; ++n) X[n] = ldb<DENSE>(b, bt, 7 + n);
  WAIT_LGKM0();
}
template <int ODD>
__device__ __forceinline__ void store_h(const f32x4 (&acc)[4][9], const float (&om)[4], rsrc_t rws, int sbase, int r, int q) {
  const int vp = ((((q & 1) << 1) | (q >> 1)) * 16 + r) * 16 + sbase + ODD * 1024;
  const int vh = ((ODD * 2 + (q >> 1)) * 16 + r) * 16 + (q & 1) * 8 + sbase + (ODD ? 0 : 4096);
#pragma unroll
  for (int m = 0; m < 4; ++m) {
    f32x4 t[9];
#pragma unroll
    for (int n = 0; n < 9; ++n)
#pragma unroll
      for (int e = 0; e < 4; ++e) t[n][e] = acc[m][n][e] * om[m];
#pragma unroll
    for (int p = 0; p < 4; ++p)
      __builtin_amdgcn_raw_buffer_store_b128(relu4(swap_pair(t[2 * p + ODD], t[2 * p + 1 + ODD])), rws,
                                             vp + m * 147456 + p * 1024, 0, AUX_SC1);
    const f32x4 s = t[ODD ? 0 : 8];
    u32x2 v = {relu2(pk(s[0], s[1])), relu2(pk(s[2], s[3]))};
    __builtin_amdgcn_raw_buffer_store_b64(v, rws, vh + m * 147456, 0, AUX_SC1);
  }
}

__device__ __forceinline__ void consumer(const Params& P, const Ids& I, AS3 char* L, int cw, int lane, unsigned tag) {
  const int wg = I.wg, wave = cw;
  (void)wg; (void)wave;
  const int r = lane & 15, q = lane >> 4;
  const rsrc_t rws = mk(P.ws, WS_END);
  if (PRIO) __builtin_amdgcn_s_setprio(PRIO);
  STAMP(0);
  asm volatile("" : "+s"(tag));
  const unsigned sink = (unsigned)(uintptr_t)(L + LDS_PF) + cw * 256;
  if (PF_X && cw == 0) touch<0>(P.x, (I.wg * 72 + lane) * 128, sink);
  if (PF_X && cw == 1)
    touch<0>(lane < 8 ? P.x : (I.wg < 128 ? P.wdr : P.wdi), 0, sink, lane < 8 ? (I.wg * 72 + 64 + lane) * 128 : min((I.wg & 127) * 41 + lane - 8, 5183) * 128);
  const int rc = min(cw * 64 + lane, 143), rf = I.wg * 18 + (rc >> 3);
  const u32x4 rv = *(const u32x4*)((const char*)P.w2 + rf * (D * 2) + 1024 + (rc & 7) * 16);
  const int aL0 = r * 128 + ((q ^ ((r >> 1) & 7)) << 4), aL1 = aL0 ^ 64;
  const int bL = (4 * q + ((lane >> 2) & 3)) * 288 + (lane & 3) * 8;
  f32x4 acc[4][9];
  {
    f32x4 lg = {0.f, 0.f, 0.f, 0.f};
#pragma unroll
    for (int n = 0; n < 9; ++n) { acc[0][n] = f32x4{0.f, 0.f, 0.f, 0.f}; asm volatile("" : "+v"(acc[0][n])); }
    asm volatile("" : "+v"(lg));
#pragma unroll
    for (int m = 1; m < 4; ++m)
#pragma unroll
      for (int n = 0; n < 9; ++n) { acc[m][n] = f32x4{0.f, 0.f, 0.f, 0.f}; asm volatile("" : "+v"(acc[m][n])); }
    bf16x8 xa, xb, w0[10], w1[10];
    BAR();
    asm volatile("" ::"v"(rv));
    STAMP(1);
    xa = ld128(L + aL0 + cw * 2048); xb = ld128(L + aL1 + cw * 2048);
#pragma unroll
    for (int n = 0; n < 9; ++n) { w0[n] = ld128(L + aL0 + 8192 + n * 2048); w1[n] = ld128(L + aL1 + 8192 + n * 2048); }
    w0[9] = ld128(L + LDS_GW + I.ra * 128 + q * 16); w1[9] = ld128(L + LDS_GW + I.ra * 128 + q * 16 + 64);
    WAIT_LGKM0();
    NOP_VALU_WRITE();
    int hs = HS;
#pragma unroll 1
    for (int st = 0; st < 9; ++st) {
      BAR();
      AS3 char* b0 = L + hs + aL0;
      AS3 char* b1 = L + hs + aL1;
      AS3 char* bg = L + LDS_GW + rot9(st + 1, I.ra) * 128 + q * 16;
      SCHED();
#pragma unroll
      for (int n = 0; n < 9; ++n) { MFMA_ACC(acc[0][n], w0[n], xa); SCHED(); w0[n] = ld128(b0 + 8192 + n * 2048); SCHED(); }
      MFMA_ACC(lg, w0[9], xa); SCHED(); w0[9] = ld128(bg); xa = ld128(b0 + cw * 2048); SCHED();
#pragma unroll
      for (int n = 0; n < 9; ++n) { MFMA_ACC(acc[0][n], w1[n], xb); SCHED(); w1[n] = ld128(b1 + 8192 + n * 2048); SCHED(); }
      MFMA_ACC(lg, w1[9], xb); SCHED(); w1[9] = ld128(bg + 64); xb = ld128(b1 + cw * 2048); SCHED();
      WAIT_LGKM0();
      hs = (hs == 3 * HS) ? 0 : hs + HS;
    }
    NOP_MFMA_READ();
    STAMP(2);
    if (cw < 3) {
      const int k = rf & 31, ro = (rf & ~31) + ((k >> 2) & 1) * 16 + (k >> 3) * 4 + (k & 3);
      __builtin_amdgcn_raw_buffer_store_b128(rv, rws, WS_W2T + (((rc & 7) >> 1) * F + ro) * 32 + (rc & 1) * 16, 0, AUX_SC1);
    }
    SCHED();
    if (q == 0 && (r >> 1) == I.pc) {
      const float g = rbf(1.f / (1.f + __expf(-rbf(lg[0]))));
      __hip_atomic_store((AS1 unsigned long long*)((AS1 char*)P.ws + WS_G) + I.rb64 * 64 + cw * 16 + r,
                         ((unsigned long long)tag << 32) | __float_as_uint(g), __ATOMIC_RELAXED, __HIP_MEMORY_SCOPE_AGENT);
    }
    const float sg = (I.pc >> 2) ? -1.f : 1.f;
    const int sl = cw * 16 + r, k = sl & 31;
    const int rho = (sl & 32) + ((k >> 2) & 1) * 16 + (k >> 3) * 4 + (k & 3);
    const int sb = WS_AB + (((I.pc >> 2) * 32 + I.rb64) * 4 + (I.pc & 3)) * BIMG;
    const int vp = rho * 256 + ((q & 1) ^ (rho & 7)) * 32 + (q >> 1) * 16;
#pragma unroll
    for (int p = 0; p < 4; ++p)
      __builtin_amdgcn_raw_buffer_store_b128(swap_pair(acc[0][2 * p] * sg, acc[0][2 * p + 1] * sg), rws, sb + (vp ^ (p * 64)), 0, AUX_SC1);
    const f32x4 s = acc[0][8] * sg;
    u32x2 v = {pk(s[0], s[1]), pk(s[2], s[3])};
    __builtin_amdgcn_raw_buffer_store_b64(v, rws, sb + 16384 + rho * 32 + q * 8, 0, AUX_SC1);
  }
  const int mh = cw >> 1, kg = cw & 1;
  bf16x8 A0[4], A1[4], B[7], X0[2], X1[2];
  {
#pragma unroll
    for (int n = 0; n < 9; ++n) { acc[0][n] = f32x4{0.f, 0.f, 0.f, 0.f}; asm volatile("" : "+v"(acc[0][n])); }
    const int a0 = aL0 + cw * 8192, a1 = aL1 + cw * 8192, b0 = bL + 32768;
    BAR();
    STAMP(3);
    int sl = 2 * BS;
    first_read<false>(A0, B, X0, L + sl + a0, L + sl + a0 + 4096, L + sl + b0, L);
    NOP_VALU_WRITE();
#pragma unroll 1
    for (int st = 0; st < 9; ++st) {
      if (st == 2) wait_vm<0>();
      unit<false>(acc, A0, A1, B, X0, X1, L + sl, a1, 4096, b0 + 9216, 0);
      BAR();
      sl = (sl == 0) ? 2 * BS : sl - BS;
      unit<false>(acc, A1, A0, B, X1, X0, L + sl, a0, 4096, b0, 0);
    }
    NOP_MFMA_READ();
    STAMP(4);
    float om[4];
#pragma unroll
    for (int m = 0; m < 4; ++m) {
      om[m] = rbf(1.f - *(AS3 float*)(L + LDS_G + (cw * 64 + m * 16 + r) * 4));
    }
    const int sb = WS_H + ((I.rb256 * 16 + cw * 4) * 144 + I.sig * 36 + ((I.j * 9) >> 1)) * 1024;
    if (I.j & 1) store_h<1>(acc, om, rws, sb, r, q);
    else store_h<0>(acc, om, rws, sb, r, q);
    STAMP(5);
  }
  {
    asm volatile("" : "+v"(lane));
    const int aS = (kg ? aL1 : aL0) + mh * 8192 + kg * 4096, aH = mh * 8192 + kg * 5120 + lane * 16;
    const int d2 = kg ? -4096 : 4096;
    const int br = kg * 32 + (lane >> 2 & 15);
    const int bb = 16384 + br * 256 + (br & 7) * 32 + (lane & 3) * 8, bt = 32768 + br * 32 + (lane & 3) * 8;
    BAR();
    STAMP(6);
    first_read<true>(A0, B, X0, L + cslot(0) + aS, L + cslot(0) + aS + d2, L + cslot(0) + bb, L + cslot(0) + bt);
    BAR();
    unit<true, true>(acc, A0, A1, B, X0, X1, L + cslot(1), aS, d2, bb, bt);
    BAR();
    unit<true>(acc, A1, A0, B, X1, X0, L + cslot(2), aS, d2, bb, bt);
#pragma unroll 1
    for (int u = 2; u < 16; u += 2) {
      if (u == 8) wait_vm<0>();
      const int s1 = cslot(u + 1), s2 = cslot(u + 2), an = u < 14 ? aS : aH;
      asm volatile("" ::"s"(s1), "s"(s2), "v"(an));
      BAR();
      unit<true>(acc, A0, A1, B, X0, X1, L + s1, aS, d2, bb, bt);
      BAR();
      unit<true>(acc, A1, A0, B, X1, X0, L + s2, an, d2, bb, bt);
    }
    NOP_MFMA_READ();
    STAMP(7);
#pragma unroll
    for (int m = 0; m < 4; ++m) {
      const float g = *(AS3 float*)(L + LDS_G + ((I.j & 1) * 128 + mh * 64 + (m ^ (2 * kg)) * 16 + (lane & 15)) * 4);
#pragma unroll
      for (int n = 0; n < 9; ++n) { acc[m][n] *= g; asm volatile("" : "+v"(acc[m][n])); }
    }
    NOP_VALU_WRITE();
#pragma unroll 1
    for (int u = 16; u < 34; u += 2) {
      const int s1 = cslot(u + 1), s2 = cslot(u + 2);
      asm volatile("" ::"s"(s1), "s"(s2));
      BAR();
      unit<true>(acc, A0, A1, B, X0, X1, L + s1, aH, d2, bb, bt);
      BAR();
      unit<true>(acc, A1, A0, B, X1, X0, L + s2, aH, d2, bb, bt);
    }
    NOP_MFMA_READ();
    STAMP(8);
  }
  {
    asm volatile("" : "+v"(lane));
    AS3 char* wr = L + cw * BIMG + lane * 16;
    AS3 char* rd = L + (cw ^ 1) * BIMG + lane * 16;
#pragma unroll
    for (int m = 0; m < 2; ++m)
#pragma unroll
      for (int n = 0; n < 9; ++n) *(AS3 f32x4*)(wr + (m * 9 + n) * 1024) = acc[2 + m][n];
    WAIT_LGKM0();
    BAR();
    STAMP(10);
#pragma unroll
    for (int m = 0; m < 2; ++m)
#pragma unroll
      for (int n = 0; n < 9; ++n) acc[m][n] += *(AS3 f32x4*)(rd + (m * 9 + n) * 1024);
    if (cw == I.sig) {
      AS3 char* kp = L + 4 * BIMG + lane * 16;
#pragma unroll
      for (int m = 0; m < 2; ++m) {
#pragma unroll
        for (int n = 0; n < 8; ++n) *(AS3 f32x4*)(kp + (m * 8 + n) * 1024) = acc[m][n];
        *(AS3 f32x4*)(kp + (16 + m) * 1024) = acc[m][8];
      }
      WAIT_LGKM0();
    } else {
      const int sb = WS_XP + ((I.loc * 8 + I.rho * 4 + cw) * 3 + ((I.sig - cw - 1) & 3)) * 9216 + lane * 16;
#pragma unroll
      for (int it = 0; it < 9; ++it) {
        const f32x4 a = it < 8 ? acc[it >> 2][(it & 3) * 2] : acc[0][8];
        const f32x4 b = it < 8 ? acc[it >> 2][(it & 3) * 2 + 1] : acc[1][8];
        u32x4 v = {min(pk(a[0], a[1]), POISON - 1), min(pk(a[2], a[3]), POISON - 1), min(pk(b[0], b[1]), POISON - 1), min(pk(b[2], b[3]), POISON - 1)};
        __builtin_amdgcn_raw_buffer_store_b128(v, rws, sb + it * 1024, 0, AUX_SC1);
      }
    }
    STAMP(9);
    BAR();
  }
}

__device__ __forceinline__ void final_items(const Params& P, const Ids& I, AS3 char* L, int w, int lane) {
  const rsrc_t rws = mk(P.ws, WS_END);
  const rsrc_t ro = mk(P.out, S * D * 2);
  const int r = lane & 15, q = lane >> 4;
  const int vb = WS_XP + I.wg * 27648 + lane * 16;
  f32x4 a = *(AS3 f32x4*)(L + 4 * BIMG + w * 2048 + lane * 16);
  f32x4 b = *(AS3 f32x4*)(L + 4 * BIMG + w * 2048 + 1024 + lane * 16);
  f32x4 c = *(AS3 f32x4*)(L + 4 * BIMG + 16384 + (w & 1) * 1024 + lane * 16);
  u32x4 p[3];
  u32x2 e[3];
#pragma unroll
  for (int i = 0; i < 3; ++i) { e[i] = u32x2{0u, 0u}; p[i] = u32x4{0u, 0u, 0u, 0u}; }
  unsigned todo = 0x7;
#pragma unroll 1
  for (unsigned spins = 0; todo && spins < SPIN_MAX; ++spins) {
#pragma unroll
    for (int i = 0; i < 3; ++i)
      if (todo >> i & 1) {
        p[i] = __builtin_amdgcn_raw_buffer_load_b128(rws, vb + i * 9216 + w * 1024, 0, AUX_SC1);
        if (w < 2) e[i] = __builtin_amdgcn_raw_buffer_load_b64(rws, vb + i * 9216 + 8192 + w * 8, 0, AUX_SC1);
      }
#pragma unroll
    for (int i = 0; i < 3; ++i)
      if (todo >> i & 1) {
        const unsigned mx = max(max(max(p[i][0], p[i][1]), max(p[i][2], p[i][3])), max(e[i][0], e[i][1]));
        if (__all(mx != POISON)) todo &= ~(1u << i);
      }
    if (todo) __builtin_amdgcn_s_sleep(POLL_SLEEP);
  }
#pragma unroll
  for (int i = 0; i < 3; ++i) {
    a += f32x4{blo(p[i][0]), bhi(p[i][0]), blo(p[i][1]), bhi(p[i][1])};
    b += f32x4{blo(p[i][2]), bhi(p[i][2]), blo(p[i][3]), bhi(p[i][3])};
    c += f32x4{blo(e[i][0]), bhi(e[i][0]), blo(e[i][1]), bhi(e[i][1])};
  }
  const int row0 = I.rb128 * 128 + I.sig * 32, col0 = I.cb * 128;
  __builtin_amdgcn_raw_buffer_store_b128(swap_pair(a, b), ro, (row0 + (w >> 2) * 16 + r) * (D * 2) + (col0 + (w & 3) * 32) * 2 + (((q & 1) << 1) | (q >> 1)) * 16, 0, 0);
  if (w < 2) {
    u32x2 v = {pk(c[0], c[1]), pk(c[2], c[3])};
    __builtin_amdgcn_raw_buffer_store_b64(v, ro, (row0 + w * 16 + r) * (D * 2) + (512 + I.cb * 16) * 2 + q * 8, 0, 0);
  }
}
}

__global__ __launch_bounds__(512, 2) void gated_mix_fnet_ffn_kernel(Params P) {
  __shared__ __attribute__((aligned(1024))) char lds[LDS_BYTES];
  AS3 char* L = (AS3 char*)lds;
  const int lane = threadIdx.x & 63;
  const int wave = __builtin_amdgcn_readfirstlane(threadIdx.x >> 6);
  Ids I;
  asm volatile("s_getpc_b64 %0" : "=s"(I.pc0));
  I.wg = blockIdx.x;
  if (wave == 0)
    touch<0>((const void*)I.pc0, min(((I.wg >> 3) * PF_CL + (lane & (PF_CL - 1))) * 128, CODE_PF_BYTES), (unsigned)(uintptr_t)(L + LDS_PF));
  I.rho = (I.wg >> 2) & 1; I.sig = I.wg & 3; I.loc = I.wg >> 3;
  I.rb64 = 16 * I.rho + (I.loc >> 1); I.pc = 2 * I.sig + (I.loc & 1);
  I.rb256 = 4 * I.rho + (I.loc >> 3); I.j = I.loc & 7;
  I.rb128 = 2 * I.rb256 + (I.j & 1); I.cb = I.j >> 1;
  I.ra = (I.rb64 & 15) % 9;
  unsigned tag = *(const __attribute__((address_space(4))) unsigned*)((uintptr_t)P.ws + WS_EPOCH) + 1u;
  if (wave < 4) consumer(P, I, L, wave, lane, tag);
  else if (wave < 6) loader<5>(P, I, L, wave - 4, lane, tag);
  else loader<4>(P, I, L, wave - 4, lane, tag);
  {
    int tz = threadIdx.x;
    asm volatile("" : "+v"(tz));
    const int w2 = __builtin_amdgcn_readfirstlane(tz >> 6);
    final_items(P, I, L, w2, tz & 63);
  }
}

extern "C" void kernel_launch(void* const* d_in, const int* in_sizes, int n_in, void* d_out, int out_size, void* d_ws,
                              size_t ws_size, hipStream_t stream) {
  Params P;
  P.x = d_in[0]; P.wsr = d_in[1]; P.wsi = d_in[2]; P.wdr = d_in[3]; P.wdi = d_in[4];
  P.w1 = d_in[6]; P.w2 = d_in[7]; P.gw = d_in[8]; P.out = d_out; P.ws = d_ws;
  gated_mix_fnet_ffn_kernel<<<256, 512, 0, stream>>>(P);
}
```
